```python
import torch

def _softmax(x, axis=-1):
    return torch.softmax(x, dim=axis)
def _gelu(x):
    return torch.nn.functional.gelu(x, approximate="tanh")
def _silu(x):
    return torch.nn.functional.silu(x)
def _sigmoid(x):
    return torch.sigmoid(x)
def _layernorm(x, axis=-1, eps=1e-5):
    m = x.mean(dim=axis, keepdim=True)
    v = x.var(dim=axis, keepdim=True, unbiased=False)
    return (x - m) / torch.sqrt(v + eps)
def _scatter_add(tgt, idx, src, axis=0):
    return tgt.clone().index_add_(axis, idx, src)
def _segment_sum(src, seg, n):
    out = torch.zeros((n,) + src.shape[1:], dtype=src.dtype)
    return out.index_add_(0, seg, src)
def _pad(x, pw):
    flat = tuple(v for pair in reversed(pw) for v in pair)
    return torch.nn.functional.pad(x, flat)

kernel_name = 'sequential_preLN__softmax_full_mha__glu_gated_bilinear__prompt'

def setup_inputs():

    _w = (lambda a: (lambda t: t.to(torch.bfloat16) if t.is_floating_point() else t)(torch.as_tensor(a)))
    def _A_setup():
        B, S, D = 1, 2048, 576
        return {
            "x": _w((lambda *s: torch.randn(*s, dtype=torch.bfloat16) * 0.05)(B, S, D)),
            "wqkv": _w((lambda *s: torch.randn(*s, dtype=torch.bfloat16) * 0.05)(D, 3 * D)),
            "wo": _w((lambda *s: torch.randn(*s, dtype=torch.bfloat16) * 0.05)(D, D)),
        }
    _A_in = _A_setup()

    def _B_setup():
        B, S, D, F = 1, 2048, 576, 4608
        return {
            "x": _w((lambda *s: torch.randn(*s, dtype=torch.bfloat16) * 0.05)(B, S, D)),
            "w_gate": _w((lambda *s: torch.randn(*s, dtype=torch.bfloat16) * 0.4)(D, F) * (D ** -0.5)),
            "w_up": _w((lambda *s: torch.randn(*s, dtype=torch.bfloat16) * 0.4)(D, F) * (D ** -0.5)),
            "w_down": _w((lambda *s: torch.randn(*s, dtype=torch.bfloat16) * 0.4)(F, D) * (F ** -0.5)),
        }
    _B_in = _B_setup()

    return {
        "A_x": _A_in["x"],
        "A_wqkv": _A_in["wqkv"],
        "A_wo": _A_in["wo"],
        "B_x": _B_in["x"],
        "B_w_gate": _B_in["w_gate"],
        "B_w_up": _B_in["w_up"],
        "B_w_down": _B_in["w_down"],
    }

def reference(A_x, A_wqkv, A_wo, B_x, B_w_gate, B_w_up, B_w_down):
    def _A_ref(x, wqkv, wo):
        B, S, D = x.shape
        H = 32; Dh = D // H
        qkv = (lambda x, *d: x.permute(*d))((x @ wqkv).reshape(B, S, 3, H, Dh), 2, 0, 3, 1, 4)
        q, k, v = qkv[0], qkv[1], qkv[2]
        scale = Dh ** -0.5
        scores = torch.einsum("bhqd,bhkd->bhqk", q, k) * scale
        p = _softmax(scores, axis=-1)
        o = torch.einsum("bhqk,bhkd->bhqd", p, v)
        return (lambda x, *d: x.permute(*d))(o, 0, 2, 1, 3).reshape(B, S, D) @ wo

    def _B_ref(x, w_gate, w_up, w_down):
        g = torch.einsum("bsd,df->bsf", x, w_gate)
        u = torch.einsum("bsd,df->bsf", x, w_up)
        h = g * u
        return torch.einsum("bsf,fd->bsd", h, w_down)

    x = A_x
    h = x + _A_ref(_layernorm(x, axis=-1), A_wqkv, A_wo)
    h2 = h + _B_ref(_layernorm(h, axis=-1), B_w_gate, B_w_up, B_w_down)
    return h2
```

```cpp
#include <hip/hip_runtime.h>
#include <hip/hip_bf16.h>

typedef float f32x2 __attribute__((ext_vector_type(2)));
typedef float f32x4 __attribute__((ext_vector_type(4)));
typedef float f32x8 __attribute__((ext_vector_type(8)));
typedef float f32x16 __attribute__((ext_vector_type(16)));
typedef __bf16 bf16x2 __attribute__((ext_vector_type(2)));
typedef __bf16 bf16x8 __attribute__((ext_vector_type(8)));
typedef __bf16 bf16x16 __attribute__((ext_vector_type(16)));
typedef short s16x4 __attribute__((ext_vector_type(4)));
typedef unsigned u32x2 __attribute__((ext_vector_type(2)));
typedef unsigned u32x4 __attribute__((ext_vector_type(4)));
typedef int i32x4 __attribute__((ext_vector_type(4)));
typedef unsigned u32x8 __attribute__((ext_vector_type(8)));
typedef unsigned u32x16 __attribute__((ext_vector_type(16)));
typedef unsigned u32x32 __attribute__((ext_vector_type(32)));
typedef float f32x32 __attribute__((ext_vector_type(32)));
#define LAS __attribute__((address_space(3)))
#define DEVI __device__ __forceinline__
#define MFMA(a, b, c) __builtin_amdgcn_mfma_f32_16x16x32_bf16(__builtin_bit_cast(bf16x8, a), __builtin_bit_cast(bf16x8, b), c, 0, 0, 0)
#define SB() __builtin_amdgcn_sched_barrier(0)
#define LBAR() do { asm volatile("s_waitcnt lgkmcnt(0)\n\ts_barrier" ::: "memory"); } while (0)

constexpr int Q_TS = 36864;
constexpr int K_TS = 2496;
constexpr int V_GS = 9344, G_TS = 36864 + 4 * V_GS;
constexpr size_t WS_Q = 0;
constexpr size_t WS_KV = WS_Q + 64 * Q_TS;
constexpr size_t WS_OT = WS_KV + 64 * G_TS;
constexpr size_t WS_HC = WS_OT + 64 * 18 * 2048;
constexpr size_t WS_MUX = WS_HC + 64 * 18 * 2048;
constexpr size_t WS_SSQ = WS_MUX + 8192;
constexpr size_t WS_WO = WS_SSQ + 37 * 8192;
constexpr size_t WS_WB = WS_WO + 18 * 18 * 2048;
constexpr size_t WS_WGU = WS_WB + 18 * 1024;
constexpr size_t WS_WD = WS_WGU + 32 * 324 * 1024;
constexpr size_t WS_HH = WS_WD + 16 * 324 * 1024;
constexpr size_t WS_IB = WS_HH + 64 * 144 * 2048;
constexpr int IB_S = 27648;
constexpr size_t WS_FL = WS_IB + 256 * IB_S;
constexpr size_t WS_FB = WS_FL, WS_FR = WS_FL + 32768, WS_FC = WS_FL + 65536, WS_FA = WS_FL + 131072;
#define APRE 4
#define WRD 3
#define WBP 12
constexpr int FLAG_BYTES = 262144;
constexpr size_t WS_PL = WS_FL + FLAG_BYTES;
constexpr size_t WS_FQ = WS_PL + 2048 * 64;
constexpr size_t WS_EP = WS_FQ + 32768;
constexpr size_t WS_END = WS_EP + 128;
static_assert(WS_END < (1u << 26), "lane-masking by range check");
constexpr int LDS_BYTES = 160 * 1024;

DEVI unsigned cvtpk(float a, float b) { f32x2 v = {a, b}; return __builtin_bit_cast(unsigned, __builtin_convertvector(v, bf16x2)); }
DEVI float bflo(unsigned u) { return __uint_as_float(u << 16); }
DEVI float bfhi(unsigned u) { return __uint_as_float(u & 0xffff0000u); }
DEVI i32x4 mkrsrc(const void* p, unsigned bytes) {
  i32x4 r; r[0] = __builtin_amdgcn_readfirstlane((int)(size_t)p); r[1] = __builtin_amdgcn_readfirstlane((int)((size_t)p >> 32)) & 0xffff; r[2] = bytes; r[3] = 0x00020000; return r; }
DEVI void dma(i32x4 rs, int voff, int soff, unsigned m) {
  asm volatile("s_mov_b32 m0, %3\n\ts_nop 0\n\tbuffer_load_dwordx4 %1, %0, %2 offen lds" ::"s"(rs), "v"(voff), "s"(soff), "s"(m) : "memory"); }
DEVI void st16(void* p, u32x4 v) { asm volatile("global_store_dwordx4 %0, %1, off sc1\n\ts_nop 1" ::"v"(p), "v"(v) : "memory"); }
DEVI void st8(void* p, u32x2 v) { asm volatile("global_store_dwordx2 %0, %1, off sc1\n\ts_nop 1" ::"v"(p), "v"(v) : "memory"); }
DEVI int lane_id() { int l; asm volatile("v_mbcnt_lo_u32_b32 %0, -1, 0\n\tv_mbcnt_hi_u32_b32 %0, -1, %0" : "=v"(l)); __builtin_assume(l >= 0 && l < 64); return l; }
DEVI char* own(const char* p) {
  size_t q; asm volatile("s_mov_b64 %0, %1" : "=s"(q) : "s"(p)); return (char*)(__attribute__((address_space(1))) char*)q; }
template <class V> DEVI V uni(V v) {
#pragma unroll
  for (int i = 0; i < (int)(sizeof(V) / 4); ++i) v[i] = __builtin_amdgcn_readfirstlane(v[i]);
  return v; }
DEVI unsigned sel(bool c, unsigned a, unsigned b) { return c ? a : b; }
constexpr unsigned OUTSIDE = 0x80000000u;
DEVI void bst16(i32x4 rs, unsigned off, u32x4 v) { asm volatile("buffer_store_dwordx4 %0, %1, %2, 0 offen sc1\n\ts_nop 1" ::"v"(v), "v"(off), "s"(rs) : "memory"); }
DEVI void bst4(i32x4 rs, unsigned off, unsigned v) { asm volatile("buffer_store_dword %0, %1, %2, 0 offen sc1" ::"v"(v), "v"(off), "s"(rs) : "memory"); }
DEVI u32x2 trread(LAS const char* p) { return __builtin_bit_cast(u32x2, __builtin_amdgcn_ds_read_tr16_b64_v4i16((LAS s16x4*)p)); }
DEVI float gsum(float v) {
  const auto w = __builtin_amdgcn_permlane16_swap(__float_as_uint(v), __float_as_uint(v), false, false); const unsigned h = __float_as_uint(__uint_as_float(w[0]) + __uint_as_float(w[1]));
  const auto z = __builtin_amdgcn_permlane32_swap(h, h, false, false); return __uint_as_float(z[0]) + __uint_as_float(z[1]); }
DEVI int wo_row(int ks, int g, int e) {
  const int hf = ks >= 9, i = ks - 9 * hf;
  return i < 8 ? 18 * (16 * hf + 2 * i + (g >> 1)) + (e & 3) + 8 * (e >> 2) + 4 * (g & 1) : 18 * (16 * hf + 4 * g + (e >> 1)) + 16 + (e & 1); }

#define QKV_ASM0 \
  "s_mov_b64 s[64:65], %[x]\n" \
  "s_mov_b32 s66, 0x24000\n" \
  "s_add_u32 s64, s64, s74\n" \
  "s_addc_u32 s65, s65, 0\n" \
  "s_mov_b32 s67, 0x20000\n" \
  "s_and_b32 s65, s65, 0xffff\n" \
  "s_mov_b32 s80, 0\n" \
  "s_add_u32 m0, s80, s76\n" \
  "s_nop 0\n" \
  "buffer_load_dwordx4 v56, s[64:67], s72 offen lds\n" \
  "s_add_u32 m0, m0, 0x400\n" \
  "s_nop 0\n" \
  "buffer_load_dwordx4 v57, s[64:67], s72 offen lds\n" \
  "s_mov_b64 s[68:69], %[w]\n" \
  "s_mov_b32 s70, 0x1e6000\n" \
  "s_mov_b32 s71, 0x20000\n" \
  "s_and_b32 s69, s69, 0xffff\n" \
  "s_add_u32 m0, s80, s77\n" \
  "s_add_u32 s82, s73, 0x80\n" \
  "buffer_load_dwordx4 v58, s[68:71], s73 offen lds\n" \
  "s_add_u32 m0, m0, 0x2000\n" \
  "s_nop 0\n" \
  "buffer_load_dwordx4 v58, s[68:71], s82 offen lds\n" \
  "s_add_u32 s72, s72, 0x80\n" \
  "s_add_u32 s73, s73, 221184\n" \
  "s_add_u32 s80, s80, 0x8000\n" \
  "s_and_b32 s80, s80, 0x18000\n" \
  "s_add_u32 m0, s80, s76\n" \
  "s_nop 0\n" \
  "buffer_load_dwordx4 v56, s[64:67], s72 offen lds\n" \
  "s_add_u32 m0, m0, 0x400\n" \
  "s_nop 0\n" \
  "buffer_load_dwordx4 v57, s[64:67], s72 offen lds\n" \
  "s_add_u32 m0, s80, s77\n" \
  "s_add_u32 s82, s73, 0x80\n" \
  "buffer_load_dwordx4 v58, s[68:71], s73 offen lds\n" \
  "s_add_u32 m0, m0, 0x2000\n" \
  "s_nop 0\n" \
  "buffer_load_dwordx4 v58, s[68:71], s82 offen lds\n" \
  "s_add_u32 s72, s72, 0x80\n" \
  "s_add_u32 s73, s73, 221184\n"
#define QKV_ASM1 \
  "s_nop 1\n" \
  "s_mov_b32 s80, 0x10000\n" \
  "s_load_dword %[tag], %[ep], 0\n" \
  "s_mov_b64 s[74:75], -1\n" \
  "v_mov_b64 v[64:65], 0\n" \
  "v_mov_b64 v[66:67], 0\n" \
  "v_mov_b64 v[68:69], 0\n" \
  "v_mov_b64 v[70:71], 0\n" \
  "v_mov_b64 v[72:73], 0\n" \
  "v_mov_b64 v[74:75], 0\n" \
  "v_mov_b64 v[76:77], 0\n" \
  "v_mov_b64 v[78:79], 0\n" \
  "v_mov_b64 v[80:81], 0\n" \
  "v_mov_b64 v[82:83], 0\n" \
  "v_mov_b64 v[84:85], 0\n" \
  "v_mov_b64 v[86:87], 0\n" \
  "v_mov_b64 v[88:89], 0\n" \
  "v_mov_b64 v[90:91], 0\n" \
  "v_mov_b64 v[92:93], 0\n" \
  "v_mov_b64 v[94:95], 0\n" \
  "v_mov_b64 v[96:97], 0\n" \
  "v_mov_b64 v[98:99], 0\n" \
  "v_mov_b64 v[100:101], 0\n" \
  "v_mov_b64 v[102:103], 0\n" \
  "v_mov_b64 v[104:105], 0\n" \
  "v_mov_b64 v[106:107], 0\n" \
  "v_mov_b32 v108, 0x3f803f80\n" \
  "v_mov_b32 v109, 0x3f803f80\n" \
  "v_mov_b32 v110, 0x3f803f80\n" \
  "v_mov_b32 v111, 0x3f803f80\n" \
  "s_add_u32 m0, s80, s76\n" \
  "s_nop 0\n" \
  "buffer_load_dwordx4 v56, s[64:67], s72 offen lds\n" \
  "s_add_u32 m0, m0, 0x400\n" \
  "s_nop 0\n" \
  "buffer_load_dwordx4 v57, s[64:67], s72 offen lds\n" \
  "s_waitcnt vmcnt(6)\n" \
  "s_barrier\n" \
  "ds_read_b128 v[112:115], v48\n" \
  "ds_read_b64_tr_b16 v[120:121], v52 offset:0\n" \
  "ds_read_b64_tr_b16 v[122:123], v52 offset:512\n" \
  "ds_read_b128 v[116:119], v49\n" \
  "ds_read_b64_tr_b16 v[124:125], v53 offset:0\n" \
  "ds_read_b64_tr_b16 v[126:127], v53 offset:512\n" \
  "ds_read_b64_tr_b16 v[128:129], v54 offset:0\n" \
  "ds_read_b64_tr_b16 v[130:131], v54 offset:512\n" \
  "ds_read_b64_tr_b16 v[132:133], v55 offset:0\n" \
  "ds_read_b64_tr_b16 v[134:135], v55 offset:512\n" \
  "s_cmp_ge_u32 s78, 9\n" \
  "s_cbranch_scc1 2f\n" \
  "1:\n" \
  "s_waitcnt lgkmcnt(0)\n" \
  "v_mfma_f32_16x16x32_bf16 v[100:103], v[108:111], v[112:115], v[100:103]\n" \
  "ds_read_b128 v[136:139], v50\n" \
  "ds_read_b64_tr_b16 v[144:145], v52 offset:4096\n" \
  "v_mfma_f32_16x16x32_bf16 v[104:107], v[112:115], v[112:115], v[104:107]\n" \
  "ds_read_b64_tr_b16 v[146:147], v52 offset:4608\n" \
  "ds_read_b128 v[140:143], v51\n" \
  "v_mfma_f32_16x16x32_bf16 v[96:99], v[120:123], v[108:111], v[96:99]\n" \
  "ds_read_b64_tr_b16 v[148:149], v53 offset:4096\n" \
  "ds_read_b64_tr_b16 v[150:151], v53 offset:4608\n" \
  "v_mfma_f32_16x16x32_bf16 v[64:67], v[120:123], v[112:115], v[64:67]\n" \
  "ds_read_b64_tr_b16 v[152:153], v54 offset:4096\n" \
  "ds_read_b64_tr_b16 v[154:155], v54 offset:4608\n" \
  "v_mfma_f32_16x16x32_bf16 v[68:71], v[120:123], v[116:119], v[68:71]\n" \
  "ds_read_b64_tr_b16 v[156:157], v55 offset:4096\n" \
  "ds_read_b64_tr_b16 v[158:159], v55 offset:4608\n" \
  "v_mfma_f32_16x16x32_bf16 v[72:75], v[124:127], v[112:115], v[72:75]\n" \
  "s_and_b32 s82, s79, 3\n" \
  "s_cmp_eq_u32 s82, 3\n" \
  "s_cselect_b32 s81, 0x20000, 0\n" \
  "s_sub_u32 s81, 0x8000, s81\n" \
  "v_mfma_f32_16x16x32_bf16 v[76:79], v[124:127], v[116:119], v[76:79]\n" \
  "s_add_u32 m0, s80, s77\n" \
  "s_add_u32 s82, s73, 0x80\n" \
  "s_mov_b64 exec, s[74:75]\n" \
  "buffer_load_dwordx4 v58, s[68:71], s73 offen lds\n" \
  "s_mov_b64 exec, -1\n" \
  "v_mfma_f32_16x16x32_bf16 v[80:83], v[128:131], v[112:115], v[80:83]\n" \
  "v_add_u32 v48, s81, v48\n" \
  "v_add_u32 v49, s81, v49\n" \
  "v_mfma_f32_16x16x32_bf16 v[84:87], v[128:131], v[116:119], v[84:87]\n" \
  "s_add_u32 m0, m0, 0x2000\n" \
  "s_mov_b64 exec, s[74:75]\n" \
  "buffer_load_dwordx4 v58, s[68:71], s82 offen lds\n" \
  "s_mov_b64 exec, -1\n" \
  "v_mfma_f32_16x16x32_bf16 v[88:91], v[132:135], v[112:115], v[88:91]\n" \
  "v_add_u32 v52, s81, v52\n" \
  "v_add_u32 v53, s81, v53\n" \
  "v_mfma_f32_16x16x32_bf16 v[92:95], v[132:135], v[116:119], v[92:95]\n" \
  "v_add_u32 v54, s81, v54\n" \
  "v_add_u32 v55, s81, v55\n" \
  "s_waitcnt vmcnt(4) lgkmcnt(0)\n" \
  "s_barrier\n" \
  "v_mfma_f32_16x16x32_bf16 v[100:103], v[108:111], v[136:139], v[100:103]\n" \
  "ds_read_b128 v[112:115], v48\n" \
  "ds_read_b64_tr_b16 v[120:121], v52 offset:0\n" \
  "v_mfma_f32_16x16x32_bf16 v[104:107], v[136:139], v[136:139], v[104:107]\n" \
  "ds_read_b64_tr_b16 v[122:123], v52 offset:512\n" \
  "ds_read_b128 v[116:119], v49\n" \
  "s_add_u32 s72, s72, 0x80\n" \
  "s_add_u32 s73, s73, 221184\n" \
  "v_mfma_f32_16x16x32_bf16 v[96:99], v[144:147], v[108:111], v[96:99]\n" \
  "ds_read_b64_tr_b16 v[124:125], v53 offset:0\n" \
  "ds_read_b64_tr_b16 v[126:127], v53 offset:512\n" \
  "s_add_u32 s80, s80, 0x8000\n" \
  "s_and_b32 s80, s80, 0x18000\n" \
  "v_mfma_f32_16x16x32_bf16 v[64:67], v[144:147], v[136:139], v[64:67]\n" \
  "ds_read_b64_tr_b16 v[128:129], v54 offset:0\n" \
  "ds_read_b64_tr_b16 v[130:131], v54 offset:512\n" \
  "v_mfma_f32_16x16x32_bf16 v[68:71], v[144:147], v[140:143], v[68:71]\n" \
  "ds_read_b64_tr_b16 v[132:133], v55 offset:0\n" \
  "ds_read_b64_tr_b16 v[134:135], v55 offset:512\n" \
  "s_cmp_ge_u32 s79, 6\n" \
  "s_cselect_b32 s66, 0, s66\n" \
  "s_cselect_b32 s70, 0, s70\n" \
  "s_cselect_b64 s[74:75], 1, -1\n" \
  "v_mfma_f32_16x16x32_bf16 v[72:75], v[148:151], v[136:139], v[72:75]\n" \
  "s_add_u32 m0, s80, s76\n" \
  "s_mov_b64 exec, s[74:75]\n" \
  "buffer_load_dwordx4 v56, s[64:67], s72 offen lds\n" \
  "s_mov_b64 exec, -1\n" \
  "v_mfma_f32_16x16x32_bf16 v[76:79], v[148:151], v[140:143], v[76:79]\n" \
  "v_add_u32 v50, s81, v50\n" \
  "v_add_u32 v51, s81, v51\n" \
  "v_mfma_f32_16x16x32_bf16 v[80:83], v[152:155], v[136:139], v[80:83]\n" \
  "s_add_u32 m0, m0, 0x400\n" \
  "s_mov_b64 exec, s[74:75]\n" \
  "buffer_load_dwordx4 v57, s[64:67], s72 offen lds\n" \
  "s_mov_b64 exec, -1\n" \
  "v_mfma_f32_16x16x32_bf16 v[84:87], v[152:155], v[140:143], v[84:87]\n" \
  "v_mfma_f32_16x16x32_bf16 v[88:91], v[156:159], v[136:139], v[88:91]\n" \
  "s_add_u32 s79, s79, 1\n" \
  "s_cmp_lg_u32 s79, 9\n" \
  "v_mfma_f32_16x16x32_bf16 v[92:95], v[156:159], v[140:143], v[92:95]\n" \
  "s_cbranch_scc1 1b\n" \
  "s_waitcnt lgkmcnt(0)\n" \
  "s_mov_b32 vcc_lo, 0xaaaaaaaa\n" \
  "s_mov_b32 vcc_hi, 0xaaaaaaaa\n" \
  "s_mov_b32 s80, 0xcccccccc\n" \
  "s_mov_b32 s81, 0xcccccccc\n" \
  "ds_write_b128 v38, v[96:99]\n" \
  "ds_write_b128 v38, v[96:99] offset:256\n" \
  "v_cndmask_b32 v112, v104, v105, vcc\n" \
  "v_cndmask_b32 v113, v106, v107, vcc\n" \
  "v_mul_f32 v114, 0x3ae38e39, v100\n" \
  "v_cndmask_b32 v112, v112, v113, s[80:81]\n" \
  "v_mul_f32 v112, 0x3ae38e39, v112\n" \
  "v_cvt_pk_bf16_f32 v115, 0, v114\n" \
  "v_fma_f32 v112, -v114, v114, v112\n" \
  "v_max_f32 v112, 0, v112\n" \
  "v_cvt_pk_bf16_f32 v112, 0, v112\n" \
  "v_add_f32 v112, 0x3727c5ac, v112\n" \
  "v_cvt_pk_bf16_f32 v112, 0, v112\n" \
  "v_sqrt_f32 v112, v112\n" \
  "ds_write_b32 v37, v115\n" \
  "v_cvt_pk_bf16_f32 v112, 0, v112\n" \
  "v_rcp_f32 v112, v112\n" \
  "buffer_store_dword v114, v39, s[56:59], 0 offen sc1\n" \
  "ds_write_b32 v37, v112 offset:4\n" \
  "s_add_u32 %[tag], %[tag], 1\n" \
  "s_waitcnt lgkmcnt(0)\n" \
  "s_barrier\n" \
  "ds_read_b64 v[112:113], v40\n" \
  "ds_read_b64 v[120:121], v41\n" \
  "ds_read_b128 v[136:139], v42 offset:0\n" \
  "ds_read_b128 v[140:143], v42 offset:64\n" \
  "ds_read_b128 v[144:147], v42 offset:128\n" \
  "ds_read_b128 v[148:151], v42 offset:192\n" \
  "s_waitcnt lgkmcnt(4)\n" \
  "v_mul_f32 v113, s62, v113\n" \
  "v_mul_f32 v121, s62, v121\n" \
  "s_waitcnt lgkmcnt(3)\n" \
  "v_pk_fma_f32 v[64:65], v[112:113], v[136:137], v[64:65] op_sel_hi:[0,1,1] neg_lo:[1,0,0] neg_hi:[1,0,0]\n" \
  "v_pk_fma_f32 v[66:67], v[112:113], v[138:139], v[66:67] op_sel_hi:[0,1,1] neg_lo:[1,0,0] neg_hi:[1,0,0]\n" \
  "v_pk_fma_f32 v[68:69], v[120:121], v[136:137], v[68:69] op_sel_hi:[0,1,1] neg_lo:[1,0,0] neg_hi:[1,0,0]\n" \
  "v_pk_fma_f32 v[70:71], v[120:121], v[138:139], v[70:71] op_sel_hi:[0,1,1] neg_lo:[1,0,0] neg_hi:[1,0,0]\n" \
  "v_pk_mul_f32 v[64:65], v[64:65], v[112:113] op_sel:[0,1] op_sel_hi:[1,1]\n" \
  "v_pk_mul_f32 v[66:67], v[66:67], v[112:113] op_sel:[0,1] op_sel_hi:[1,1]\n" \
  "v_pk_mul_f32 v[68:69], v[68:69], v[120:121] op_sel:[0,1] op_sel_hi:[1,1]\n" \
  "v_pk_mul_f32 v[70:71], v[70:71], v[120:121] op_sel:[0,1] op_sel_hi:[1,1]\n" \
  "v_cvt_pk_bf16_f32 v64, v64, v65\n" \
  "v_cvt_pk_bf16_f32 v65, v66, v67\n" \
  "v_cvt_pk_bf16_f32 v66, v68, v69\n" \
  "v_cvt_pk_bf16_f32 v67, v70, v71\n" \
  "s_mov_b64 exec, s[60:61]\n" \
  "v_swap_b32 v64, v66\n" \
  "v_swap_b32 v65, v67\n" \
  "s_mov_b64 exec, -1\n" \
  "buffer_store_dwordx4 v[64:67], v32, s[56:59], 0 offen sc1\n" \
  "s_waitcnt lgkmcnt(2)\n" \
  "v_pk_fma_f32 v[72:73], v[112:113], v[140:141], v[72:73] op_sel_hi:[0,1,1] neg_lo:[1,0,0] neg_hi:[1,0,0]\n" \
  "v_pk_fma_f32 v[74:75], v[112:113], v[142:143], v[74:75] op_sel_hi:[0,1,1] neg_lo:[1,0,0] neg_hi:[1,0,0]\n" \
  "v_pk_fma_f32 v[76:77], v[120:121], v[140:141], v[76:77] op_sel_hi:[0,1,1] neg_lo:[1,0,0] neg_hi:[1,0,0]\n" \
  "v_pk_fma_f32 v[78:79], v[120:121], v[142:143], v[78:79] op_sel_hi:[0,1,1] neg_lo:[1,0,0] neg_hi:[1,0,0]\n" \
  "v_pk_mul_f32 v[72:73], v[72:73], v[112:113] op_sel:[0,1] op_sel_hi:[1,1]\n" \
  "v_pk_mul_f32 v[74:75], v[74:75], v[112:113] op_sel:[0,1] op_sel_hi:[1,1]\n" \
  "v_pk_mul_f32 v[76:77], v[76:77], v[120:121] op_sel:[0,1] op_sel_hi:[1,1]\n" \
  "v_pk_mul_f32 v[78:79], v[78:79], v[120:121] op_sel:[0,1] op_sel_hi:[1,1]\n" \
  "v_cvt_pk_bf16_f32 v72, v72, v73\n" \
  "v_cvt_pk_bf16_f32 v73, v74, v75\n" \
  "v_cvt_pk_bf16_f32 v74, v76, v77\n" \
  "v_cvt_pk_bf16_f32 v75, v78, v79\n" \
  "s_mov_b64 exec, s[60:61]\n" \
  "v_swap_b32 v72, v74\n" \
  "v_swap_b32 v73, v75\n" \
  "s_mov_b64 exec, -1\n" \
  "buffer_store_dwordx4 v[72:75], v33, s[56:59], 0 offen sc1\n" \
  "s_waitcnt lgkmcnt(1)\n" \
  "v_pk_fma_f32 v[80:81], v[112:113], v[144:145], v[80:81] op_sel_hi:[0,1,1] neg_lo:[1,0,0] neg_hi:[1,0,0]\n" \
  "v_pk_fma_f32 v[82:83], v[112:113], v[146:147], v[82:83] op_sel_hi:[0,1,1] neg_lo:[1,0,0] neg_hi:[1,0,0]\n" \
  "v_pk_fma_f32 v[84:85], v[120:121], v[144:145], v[84:85] op_sel_hi:[0,1,1] neg_lo:[1,0,0] neg_hi:[1,0,0]\n" \
  "v_pk_fma_f32 v[86:87], v[120:121], v[146:147], v[86:87] op_sel_hi:[0,1,1] neg_lo:[1,0,0] neg_hi:[1,0,0]\n" \
  "v_pk_mul_f32 v[80:81], v[80:81], v[112:113] op_sel:[0,1] op_sel_hi:[1,1]\n" \
  "v_pk_mul_f32 v[82:83], v[82:83], v[112:113] op_sel:[0,1] op_sel_hi:[1,1]\n" \
  "v_pk_mul_f32 v[84:85], v[84:85], v[120:121] op_sel:[0,1] op_sel_hi:[1,1]\n" \
  "v_pk_mul_f32 v[86:87], v[86:87], v[120:121] op_sel:[0,1] op_sel_hi:[1,1]\n" \
  "v_cvt_pk_bf16_f32 v80, v80, v81\n" \
  "v_cvt_pk_bf16_f32 v81, v82, v83\n" \
  "v_cvt_pk_bf16_f32 v82, v84, v85\n" \
  "v_cvt_pk_bf16_f32 v83, v86, v87\n" \
  "s_mov_b64 exec, s[60:61]\n" \
  "v_swap_b32 v80, v82\n" \
  "v_swap_b32 v81, v83\n" \
  "s_mov_b64 exec, -1\n" \
  "buffer_store_dwordx4 v[80:83], v34, s[56:59], 0 offen sc1\n" \
  "s_waitcnt lgkmcnt(0)\n" \
  "v_pk_fma_f32 v[88:89], v[112:113], v[148:149], v[88:89] op_sel_hi:[0,1,1] neg_lo:[1,0,0] neg_hi:[1,0,0]\n" \
  "v_pk_fma_f32 v[90:91], v[112:113], v[150:151], v[90:91] op_sel_hi:[0,1,1] neg_lo:[1,0,0] neg_hi:[1,0,0]\n" \
  "v_pk_fma_f32 v[92:93], v[120:121], v[148:149], v[92:93] op_sel_hi:[0,1,1] neg_lo:[1,0,0] neg_hi:[1,0,0]\n" \
  "v_pk_fma_f32 v[94:95], v[120:121], v[150:151], v[94:95] op_sel_hi:[0,1,1] neg_lo:[1,0,0] neg_hi:[1,0,0]\n" \
  "v_pk_mul_f32 v[88:89], v[88:89], v[112:113] op_sel:[0,1] op_sel_hi:[1,1]\n" \
  "v_pk_mul_f32 v[90:91], v[90:91], v[112:113] op_sel:[0,1] op_sel_hi:[1,1]\n" \
  "v_pk_mul_f32 v[92:93], v[92:93], v[120:121] op_sel:[0,1] op_sel_hi:[1,1]\n" \
  "v_pk_mul_f32 v[94:95], v[94:95], v[120:121] op_sel:[0,1] op_sel_hi:[1,1]\n" \
  "v_cvt_pk_bf16_f32 v88, v88, v89\n" \
  "v_cvt_pk_bf16_f32 v89, v90, v91\n" \
  "v_cvt_pk_bf16_f32 v90, v92, v93\n" \
  "v_cvt_pk_bf16_f32 v91, v94, v95\n" \
  "s_mov_b64 exec, s[60:61]\n" \
  "v_swap_b32 v88, v90\n" \
  "v_swap_b32 v89, v91\n" \
  "s_mov_b64 exec, -1\n" \
  "buffer_store_dwordx4 v[88:91], v35, s[56:59], 0 offen sc1\n" \
  "s_branch 3f\n" \
  "2:\n" \
  "s_waitcnt lgkmcnt(0)\n" \
  "v_mfma_f32_16x16x32_bf16 v[100:103], v[108:111], v[112:115], v[100:103]\n" \
  "ds_read_b128 v[136:139], v50\n" \
  "ds_read_b64_tr_b16 v[144:145], v52 offset:4096\n" \
  "v_mfma_f32_16x16x32_bf16 v[104:107], v[112:115], v[112:115], v[104:107]\n" \
  "ds_read_b64_tr_b16 v[146:147], v52 offset:4608\n" \
  "ds_read_b128 v[140:143], v51\n" \
  "v_mfma_f32_16x16x32_bf16 v[96:99], v[120:123], v[108:111], v[96:99]\n" \
  "ds_read_b64_tr_b16 v[148:149], v53 offset:4096\n" \
  "ds_read_b64_tr_b16 v[150:151], v53 offset:4608\n" \
  "v_mfma_f32_16x16x32_bf16 v[64:67], v[112:115], v[120:123], v[64:67]\n" \
  "ds_read_b64_tr_b16 v[152:153], v54 offset:4096\n" \
  "ds_read_b64_tr_b16 v[154:155], v54 offset:4608\n" \
  "v_mfma_f32_16x16x32_bf16 v[68:71], v[116:119], v[120:123], v[68:71]\n" \
  "ds_read_b64_tr_b16 v[156:157], v55 offset:4096\n" \
  "ds_read_b64_tr_b16 v[158:159], v55 offset:4608\n" \
  "v_mfma_f32_16x16x32_bf16 v[72:75], v[112:115], v[124:127], v[72:75]\n" \
  "s_and_b32 s82, s79, 3\n" \
  "s_cmp_eq_u32 s82, 3\n" \
  "s_cselect_b32 s81, 0x20000, 0\n" \
  "s_sub_u32 s81, 0x8000, s81\n" \
  "v_mfma_f32_16x16x32_bf16 v[76:79], v[116:119], v[124:127], v[76:79]\n" \
  "s_add_u32 m0, s80, s77\n" \
  "s_add_u32 s82, s73, 0x80\n" \
  "s_mov_b64 exec, s[74:75]\n" \
  "buffer_load_dwordx4 v58, s[68:71], s73 offen lds\n" \
  "s_mov_b64 exec, -1\n" \
  "v_mfma_f32_16x16x32_bf16 v[80:83], v[112:115], v[128:131], v[80:83]\n" \
  "v_add_u32 v48, s81, v48\n" \
  "v_add_u32 v49, s81, v49\n" \
  "v_mfma_f32_16x16x32_bf16 v[84:87], v[116:119], v[128:131], v[84:87]\n" \
  "s_add_u32 m0, m0, 0x2000\n" \
  "s_mov_b64 exec, s[74:75]\n" \
  "buffer_load_dwordx4 v58, s[68:71], s82 offen lds\n" \
  "s_mov_b64 exec, -1\n" \
  "v_mfma_f32_16x16x32_bf16 v[88:91], v[112:115], v[132:135], v[88:91]\n" \
  "v_add_u32 v52, s81, v52\n" \
  "v_add_u32 v53, s81, v53\n" \
  "v_mfma_f32_16x16x32_bf16 v[92:95], v[116:119], v[132:135], v[92:95]\n" \
  "v_add_u32 v54, s81, v54\n" \
  "v_add_u32 v55, s81, v55\n" \
  "s_waitcnt vmcnt(4) lgkmcnt(0)\n" \
  "s_barrier\n" \
  "v_mfma_f32_16x16x32_bf16 v[100:103], v[108:111], v[136:139], v[100:103]\n" \
  "ds_read_b128 v[112:115], v48\n" \
  "ds_read_b64_tr_b16 v[120:121], v52 offset:0\n" \
  "v_mfma_f32_16x16x32_bf16 v[104:107], v[136:139], v[136:139], v[104:107]\n" \
  "ds_read_b64_tr_b16 v[122:123], v52 offset:512\n" \
  "ds_read_b128 v[116:119], v49\n" \
  "s_add_u32 s72, s72, 0x80\n" \
  "s_add_u32 s73, s73, 221184\n" \
  "v_mfma_f32_16x16x32_bf16 v[96:99], v[144:147], v[108:111], v[96:99]\n" \
  "ds_read_b64_tr_b16 v[124:125], v53 offset:0\n" \
  "ds_read_b64_tr_b16 v[126:127], v53 offset:512\n" \
  "s_add_u32 s80, s80, 0x8000\n" \
  "s_and_b32 s80, s80, 0x18000\n" \
  "v_mfma_f32_16x16x32_bf16 v[64:67], v[136:139], v[144:147], v[64:67]\n" \
  "ds_read_b64_tr_b16 v[128:129], v54 offset:0\n" \
  "ds_read_b64_tr_b16 v[130:131], v54 offset:512\n" \
  "v_mfma_f32_16x16x32_bf16 v[68:71], v[140:143], v[144:147], v[68:71]\n" \
  "ds_read_b64_tr_b16 v[132:133], v55 offset:0\n" \
  "ds_read_b64_tr_b16 v[134:135], v55 offset:512\n" \
  "s_cmp_ge_u32 s79, 6\n" \
  "s_cselect_b32 s66, 0, s66\n" \
  "s_cselect_b32 s70, 0, s70\n" \
  "s_cselect_b64 s[74:75], 1, -1\n" \
  "v_mfma_f32_16x16x32_bf16 v[72:75], v[136:139], v[148:151], v[72:75]\n" \
  "s_add_u32 m0, s80, s76\n" \
  "s_mov_b64 exec, s[74:75]\n" \
  "buffer_load_dwordx4 v56, s[64:67], s72 offen lds\n" \
  "s_mov_b64 exec, -1\n" \
  "v_mfma_f32_16x16x32_bf16 v[76:79], v[140:143], v[148:151], v[76:79]\n" \
  "v_add_u32 v50, s81, v50\n" \
  "v_add_u32 v51, s81, v51\n" \
  "v_mfma_f32_16x16x32_bf16 v[80:83], v[136:139], v[152:155], v[80:83]\n" \
  "s_add_u32 m0, m0, 0x400\n" \
  "s_mov_b64 exec, s[74:75]\n" \
  "buffer_load_dwordx4 v57, s[64:67], s72 offen lds\n" \
  "s_mov_b64 exec, -1\n" \
  "v_mfma_f32_16x16x32_bf16 v[84:87], v[140:143], v[152:155], v[84:87]\n" \
  "v_mfma_f32_16x16x32_bf16 v[88:91], v[136:139], v[156:159], v[88:91]\n" \
  "s_add_u32 s79, s79, 1\n" \
  "s_cmp_lg_u32 s79, 9\n" \
  "v_mfma_f32_16x16x32_bf16 v[92:95], v[140:143], v[156:159], v[92:95]\n" \
  "s_cbranch_scc1 2b\n" \
  "s_waitcnt lgkmcnt(0)\n" \
  "s_mov_b32 vcc_lo, 0xaaaaaaaa\n" \
  "s_mov_b32 vcc_hi, 0xaaaaaaaa\n" \
  "s_mov_b32 s80, 0xcccccccc\n" \
  "s_mov_b32 s81, 0xcccccccc\n" \
  "ds_write_b128 v38, v[96:99]\n" \
  "ds_write_b128 v38, v[96:99] offset:256\n" \
  "v_cndmask_b32 v112, v104, v105, vcc\n" \
  "v_cndmask_b32 v113, v106, v107, vcc\n" \
  "v_mul_f32 v114, 0x3ae38e39, v100\n" \
  "v_cndmask_b32 v112, v112, v113, s[80:81]\n" \
  "v_mul_f32 v112, 0x3ae38e39, v112\n" \
  "v_cvt_pk_bf16_f32 v115, 0, v114\n" \
  "v_fma_f32 v112, -v114, v114, v112\n" \
  "v_max_f32 v112, 0, v112\n" \
  "v_cvt_pk_bf16_f32 v112, 0, v112\n" \
  "v_add_f32 v112, 0x3727c5ac, v112\n" \
  "v_cvt_pk_bf16_f32 v112, 0, v112\n" \
  "v_sqrt_f32 v112, v112\n" \
  "ds_write_b32 v37, v115\n" \
  "v_cvt_pk_bf16_f32 v112, 0, v112\n" \
  "v_rcp_f32 v112, v112\n" \
  "buffer_store_dword v114, v39, s[56:59], 0 offen sc1\n" \
  "ds_write_b32 v37, v112 offset:512\n" \
  "s_add_u32 %[tag], %[tag], 1\n" \
  "s_waitcnt lgkmcnt(0)\n" \
  "s_barrier\n" \
  "ds_read_b128 v[112:115], v40\n" \
  "ds_read_b128 v[120:123], v41\n" \
  "ds_read_b32 v136, v42\n" \
  "ds_read_b128 v[116:119], v40 offset:512\n" \
  "ds_read_b128 v[124:127], v41 offset:512\n" \
  "ds_read_b32 v140, v42 offset:64\n" \
  "ds_read_b32 v144, v42 offset:128\n" \
  "ds_read_b32 v148, v42 offset:192\n" \
  "s_waitcnt lgkmcnt(3)\n" \
  "v_pk_fma_f32 v[64:65], v[112:113], v[136:137], v[64:65] op_sel_hi:[1,0,1] neg_lo:[1,0,0] neg_hi:[1,0,0]\n" \
  "v_pk_fma_f32 v[66:67], v[114:115], v[136:137], v[66:67] op_sel_hi:[1,0,1] neg_lo:[1,0,0] neg_hi:[1,0,0]\n" \
  "v_pk_fma_f32 v[68:69], v[120:121], v[136:137], v[68:69] op_sel_hi:[1,0,1] neg_lo:[1,0,0] neg_hi:[1,0,0]\n" \
  "v_pk_fma_f32 v[70:71], v[122:123], v[136:137], v[70:71] op_sel_hi:[1,0,1] neg_lo:[1,0,0] neg_hi:[1,0,0]\n" \
  "v_pk_mul_f32 v[64:65], v[64:65], v[116:117]\n" \
  "v_pk_mul_f32 v[66:67], v[66:67], v[118:119]\n" \
  "v_pk_mul_f32 v[68:69], v[68:69], v[124:125]\n" \
  "v_pk_mul_f32 v[70:71], v[70:71], v[126:127]\n" \
  "v_cvt_pk_bf16_f32 v64, v64, v65\n" \
  "v_cvt_pk_bf16_f32 v65, v66, v67\n" \
  "v_cvt_pk_bf16_f32 v66, v68, v69\n" \
  "v_cvt_pk_bf16_f32 v67, v70, v71\n" \
  "s_mov_b64 exec, s[60:61]\n" \
  "v_swap_b32 v64, v66\n" \
  "v_swap_b32 v65, v67\n" \
  "s_mov_b64 exec, -1\n" \
  "buffer_store_dwordx4 v[64:67], v32, s[56:59], 0 offen sc1\n" \
  "s_waitcnt lgkmcnt(2)\n" \
  "v_pk_fma_f32 v[72:73], v[112:113], v[140:141], v[72:73] op_sel_hi:[1,0,1] neg_lo:[1,0,0] neg_hi:[1,0,0]\n" \
  "v_pk_fma_f32 v[74:75], v[114:115], v[140:141], v[74:75] op_sel_hi:[1,0,1] neg_lo:[1,0,0] neg_hi:[1,0,0]\n" \
  "v_pk_fma_f32 v[76:77], v[120:121], v[140:141], v[76:77] op_sel_hi:[1,0,1] neg_lo:[1,0,0] neg_hi:[1,0,0]\n" \
  "v_pk_fma_f32 v[78:79], v[122:123], v[140:141], v[78:79] op_sel_hi:[1,0,1] neg_lo:[1,0,0] neg_hi:[1,0,0]\n" \
  "v_pk_mul_f32 v[72:73], v[72:73], v[116:117]\n" \
  "v_pk_mul_f32 v[74:75], v[74:75], v[118:119]\n" \
  "v_pk_mul_f32 v[76:77], v[76:77], v[124:125]\n" \
  "v_pk_mul_f32 v[78:79], v[78:79], v[126:127]\n" \
  "v_cvt_pk_bf16_f32 v72, v72, v73\n" \
  "v_cvt_pk_bf16_f32 v73, v74, v75\n" \
  "v_cvt_pk_bf16_f32 v74, v76, v77\n" \
  "v_cvt_pk_bf16_f32 v75, v78, v79\n" \
  "s_mov_b64 exec, s[60:61]\n" \
  "v_swap_b32 v72, v74\n" \
  "v_swap_b32 v73, v75\n" \
  "s_mov_b64 exec, -1\n" \
  "buffer_store_dwordx4 v[72:75], v33, s[56:59], 0 offen sc1\n" \
  "s_waitcnt lgkmcnt(1)\n" \
  "v_pk_fma_f32 v[80:81], v[112:113], v[144:145], v[80:81] op_sel_hi:[1,0,1] neg_lo:[1,0,0] neg_hi:[1,0,0]\n" \
  "v_pk_fma_f32 v[82:83], v[114:115], v[144:145], v[82:83] op_sel_hi:[1,0,1] neg_lo:[1,0,0] neg_hi:[1,0,0]\n" \
  "v_pk_fma_f32 v[84:85], v[120:121], v[144:145], v[84:85] op_sel_hi:[1,0,1] neg_lo:[1,0,0] neg_hi:[1,0,0]\n" \
  "v_pk_fma_f32 v[86:87], v[122:123], v[144:145], v[86:87] op_sel_hi:[1,0,1] neg_lo:[1,0,0] neg_hi:[1,0,0]\n" \
  "v_pk_mul_f32 v[80:81], v[80:81], v[116:117]\n" \
  "v_pk_mul_f32 v[82:83], v[82:83], v[118:119]\n" \
  "v_pk_mul_f32 v[84:85], v[84:85], v[124:125]\n" \
  "v_pk_mul_f32 v[86:87], v[86:87], v[126:127]\n" \
  "v_cvt_pk_bf16_f32 v80, v80, v81\n" \
  "v_cvt_pk_bf16_f32 v81, v82, v83\n" \
  "v_cvt_pk_bf16_f32 v82, v84, v85\n" \
  "v_cvt_pk_bf16_f32 v83, v86, v87\n" \
  "s_mov_b64 exec, s[60:61]\n" \
  "v_swap_b32 v80, v82\n" \
  "v_swap_b32 v81, v83\n" \
  "s_mov_b64 exec, -1\n" \
  "buffer_store_dwordx4 v[80:83], v34, s[56:59], 0 offen sc1\n" \
  "s_waitcnt lgkmcnt(0)\n" \
  "v_pk_fma_f32 v[88:89], v[112:113], v[148:149], v[88:89] op_sel_hi:[1,0,1] neg_lo:[1,0,0] neg_hi:[1,0,0]\n" \
  "v_pk_fma_f32 v[90:91], v[114:115], v[148:149], v[90:91] op_sel_hi:[1,0,1] neg_lo:[1,0,0] neg_hi:[1,0,0]\n" \
  "v_pk_fma_f32 v[92:93], v[120:121], v[148:149], v[92:93] op_sel_hi:[1,0,1] neg_lo:[1,0,0] neg_hi:[1,0,0]\n" \
  "v_pk_fma_f32 v[94:95], v[122:123], v[148:149], v[94:95] op_sel_hi:[1,0,1] neg_lo:[1,0,0] neg_hi:[1,0,0]\n" \
  "v_pk_mul_f32 v[88:89], v[88:89], v[116:117]\n" \
  "v_pk_mul_f32 v[90:91], v[90:91], v[118:119]\n" \
  "v_pk_mul_f32 v[92:93], v[92:93], v[124:125]\n" \
  "v_pk_mul_f32 v[94:95], v[94:95], v[126:127]\n" \
  "v_cvt_pk_bf16_f32 v88, v88, v89\n" \
  "v_cvt_pk_bf16_f32 v89, v90, v91\n" \
  "v_cvt_pk_bf16_f32 v90, v92, v93\n" \
  "v_cvt_pk_bf16_f32 v91, v94, v95\n" \
  "s_mov_b64 exec, s[60:61]\n" \
  "v_swap_b32 v88, v90\n" \
  "v_swap_b32 v89, v91\n" \
  "s_mov_b64 exec, -1\n" \
  "buffer_store_dwordx4 v[88:91], v35, s[56:59], 0 offen sc1\n" \
  "3:\n" \
  "s_nop 1\n"
#define QKV_CLOB "v64","v65","v66","v67","v68","v69","v70","v71","v72","v73","v74","v75","v76","v77","v78","v79","v80","v81","v82","v83","v84","v85","v86","v87","v88","v89","v90","v91","v92","v93","v94","v95","v96","v97","v98","v99","v100","v101","v102","v103","v104","v105","v106","v107","v108","v109","v110","v111","v112","v113","v114","v115","v116","v117","v118","v119","v120","v121","v122","v123","v124","v125","v126","v127","v128","v129","v130","v131","v132","v133","v134","v135","v136","v137","v138","v139","v140","v141","v142","v143","v144","v145","v146","v147","v148","v149","v150","v151","v152","v153","v154","v155","v156","v157","v158","v159","s80","s81","s82","vcc"
#define ATTN_ASM \
  "s_nop 4\n" \
  "s_mov_b32 m0, s75\n" \
  "s_cmp_eq_u32 s99, 0\n" \
  "s_cbranch_scc1 83f\n" \
  "s_mov_b32 s80, 0x40000\n" \
  "s_sleep 6\n" \
  "s_cmp_eq_u32 s99, 1\n" \
  "s_cbranch_scc1 82f\n" \
  "81:\n" \
  "buffer_load_dwordx2 v[6:7], v14, s[60:63], 0 offen sc1\n" \
  "s_sub_u32 s80, s80, 1\n" \
  "s_waitcnt vmcnt(0)\n" \
  "s_cbranch_scc1 83f\n" \
  "v_cmp_ne_u64 vcc, v[4:5], v[6:7]\n" \
  "s_cbranch_vccz 82f\n" \
  "s_sleep 1\n" \
  "s_branch 81b\n" \
  "82:\n" \
  "buffer_load_dwordx2 v[6:7], v15, s[60:63], 0 offen sc1\n" \
  "s_sub_u32 s80, s80, 1\n" \
  "s_waitcnt vmcnt(0)\n" \
  "s_cbranch_scc1 83f\n" \
  "v_cmp_ne_u64 vcc, v[4:5], v[6:7]\n" \
  "s_cbranch_vccz 83f\n" \
  "s_sleep 1\n" \
  "s_branch 82b\n" \
  "83:\n" \
  "s_barrier\n" \
  "buffer_load_dwordx4 v16, s[60:63], s58 offen sc1 lds\n" \
  "s_mov_b64 exec, 0xffff\n" \
  "buffer_load_dwordx4 v16, s[60:63], s58 offen offset:1024 sc1 lds\n" \
  "s_mov_b64 exec, -1\n" \
  "s_mov_b32 m0, s64\n" \
  "s_nop 0\n" \
  "buffer_load_dwordx4 v11, s[60:63], s70 offen offset:0 sc1 lds\n" \
  "buffer_load_dwordx4 v12, s[60:63], s70 offen offset:1024 sc1 lds\n" \
  "s_mov_b64 exec, 0xfffffff\n" \
  "buffer_load_dwordx4 v13, s[60:63], s70 offen offset:2048 sc1 lds\n" \
  "s_mov_b64 exec, -1\n" \
  "buffer_store_dword v4, v20, s[60:63], 0 offen sc1\n" \
  "v_mov_b32 v250, 0xdddd\n" \
  "v_mov_b32 v229, 0x3f80\n" \
  "v_mov_b32 v231, 0x3f80\n" \
  "v_mov_b32 v249, 0\n" \
  "v_add_u32 v251, s72, v17\n" \
  "v_add_u32 v252, s72, v18\n" \
  "v_add_u32 v253, s72, v19\n" \
  "v_mov_b64 v[192:193], 0\n" \
  "v_mov_b64 v[194:195], 0\n" \
  "v_mov_b64 v[196:197], 0\n" \
  "v_mov_b64 v[198:199], 0\n" \
  "v_mov_b64 v[200:201], 0\n" \
  "v_mov_b64 v[202:203], 0\n" \
  "v_mov_b64 v[204:205], 0\n" \
  "v_mov_b64 v[206:207], 0\n" \
  "v_mov_b64 v[208:209], 0\n" \
  "v_mov_b64 v[210:211], 0\n" \
  "v_mov_b64 v[212:213], 0\n" \
  "v_mov_b64 v[214:215], 0\n" \
  "v_mov_b64 v[216:217], 0\n" \
  "v_mov_b64 v[218:219], 0\n" \
  "v_mov_b64 v[220:221], 0\n" \
  "v_mov_b64 v[222:223], 0\n" \
  "v_mov_b64 v[236:237], 0\n" \
  "v_mov_b64 v[238:239], 0\n" \
  "s_waitcnt vmcnt(1)\n" \
  "s_barrier\n" \
  "ds_read_b32 v228, v18 offset:0\n" \
  "ds_read_b32 v248, v10\n" \
  "ds_read_b64 v[224:225], v17 offset:0\n" \
  "ds_read_b64 v[226:227], v17 offset:256\n" \
  "ds_read_b64 v[240:241], v8\n" \
  "ds_read_b64 v[242:243], v8 offset:256\n" \
  "ds_read_b64 v[244:245], v9\n" \
  "ds_read_b64 v[246:247], v9 offset:256\n" \
  "s_add_u32 m0, m0, 19968\n" \
  "s_add_u32 s70, s70, 593920\n" \
  "buffer_load_dwordx4 v11, s[60:63], s70 offen offset:0 sc1 lds\n" \
  "s_waitcnt lgkmcnt(6)\n" \
  "v_mfma_f32_32x32x4_2b_bf16 v[160:191], v[228:229], v[248:249], 0\n" \
  "ds_read_b32 v228, v252 offset:0\n" \
  "ds_read_b32 v230, v252 offset:4992\n" \
  "buffer_load_dwordx4 v12, s[60:63], s70 offen offset:1024 sc1 lds\n" \
  "s_mov_b64 exec, 0xfffffff\n" \
  "buffer_load_dwordx4 v13, s[60:63], s70 offen offset:2048 sc1 lds\n" \
  "s_mov_b64 exec, -1\n" \
  "s_waitcnt lgkmcnt(2)\n" \
  "s_nop 12\n" \
  "v_mfma_f32_32x32x16_bf16 v[160:175], v[224:227], v[240:243], v[160:175]\n" \
  "v_mfma_f32_32x32x16_bf16 v[176:191], v[224:227], v[244:247], v[176:191]\n" \
  "ds_read_b64 v[224:225], v251 offset:0\n" \
  "ds_read_b64 v[226:227], v251 offset:256\n" \
  "ds_read_b128 v[232:235], v253 offset:1280\n" \
  "s_nop 8\n" \
  "v_max3_f32 v254, v160, v161, v162\n" \
  "v_max3_f32 v254, v254, v163, v164\n" \
  "v_max3_f32 v254, v254, v165, v166\n" \
  "v_max3_f32 v254, v254, v167, v168\n" \
  "v_max3_f32 v254, v254, v169, v170\n" \
  "v_max3_f32 v254, v254, v171, v172\n" \
  "v_max3_f32 v254, v254, v173, v174\n" \
  "v_max_f32 v254, v254, v175\n" \
  "v_max3_f32 v255, v176, v177, v178\n" \
  "v_max3_f32 v255, v255, v179, v180\n" \
  "v_max3_f32 v255, v255, v181, v182\n" \
  "v_max3_f32 v255, v255, v183, v184\n" \
  "v_max3_f32 v255, v255, v185, v186\n" \
  "v_max3_f32 v255, v255, v187, v188\n" \
  "v_max3_f32 v255, v255, v189, v190\n" \
  "v_max_f32 v255, v255, v191\n" \
  "s_nop 1\n" \
  "v_permlane32_swap_b32 v254, v255\n" \
  "s_nop 1\n" \
  "v_max_f32 v254, v254, v255\n" \
  "v_cvt_pk_bf16_f32 v249, -v254, 0\n" \
  "s_waitcnt lgkmcnt(0)\n" \
  "s_nop 1\n" \
  "v_mfma_f32_32x32x4_2b_bf16 v[128:159], v[228:229], v[248:249], 0\n" \
  "ds_read_b32 v228, v252 offset:9984\n" \
  "s_add_u32 m0, m0, 19968\n" \
  "s_add_u32 s70, s70, 593920\n" \
  "buffer_load_dwordx4 v11, s[60:63], s70 offen offset:0 sc1 lds\n" \
  "buffer_load_dwordx4 v12, s[60:63], s70 offen offset:1024 sc1 lds\n" \
  "s_nop 13\n" \
  "v_mfma_f32_32x32x16_bf16 v[128:143], v[224:227], v[240:243], v[128:143]\n" \
  "v_mfma_f32_32x32x16_bf16 v[144:159], v[224:227], v[244:247], v[144:159]\n" \
  "ds_read_b64 v[224:225], v251 offset:4992\n" \
  "ds_read_b64 v[226:227], v251 offset:5248\n" \
  "s_nop 7\n" \
  "s_nop 1\n" \
  "v_exp_f32 v128, v128\n" \
  "v_exp_f32 v129, v129\n" \
  "v_smfmac_f32_32x32x32_bf16 v[208:223], v[236:239], v[184:191], v250\n" \
  "ds_read_b128 v[236:239], v253 offset:1888\n" \
  "v_exp_f32 v130, v130\n" \
  "v_exp_f32 v131, v131\n" \
  "v_exp_f32 v132, v132\n" \
  "v_exp_f32 v133, v133\n" \
  "v_mfma_f32_32x32x4_2b_bf16 v[160:191], v[230:231], v[248:249], 0\n" \
  "ds_read_b32 v230, v252 offset:14976\n" \
  "v_exp_f32 v134, v134\n" \
  "v_exp_f32 v135, v135\n" \
  "v_exp_f32 v136, v136\n" \
  "v_exp_f32 v137, v137\n" \
  "v_exp_f32 v138, v138\n" \
  "v_exp_f32 v139, v139\n" \
  "v_exp_f32 v140, v140\n" \
  "v_exp_f32 v141, v141\n" \
  "s_waitcnt lgkmcnt(1)\n" \
  "v_smfmac_f32_32x32x32_bf16 v[192:207], v[232:235], v[128:135], v250\n" \
  "s_mov_b64 exec, 0xfffffff\n" \
  "buffer_load_dwordx4 v13, s[60:63], s70 offen offset:2048 sc1 lds\n" \
  "s_mov_b64 exec, -1\n" \
  "global_load_dword v22, v22, s[68:69] sc1\n" \
  "v_exp_f32 v142, v142\n" \
  "v_exp_f32 v143, v143\n" \
  "v_exp_f32 v144, v144\n" \
  "v_exp_f32 v145, v145\n" \
  "v_smfmac_f32_32x32x32_bf16 v[192:207], v[236:239], v[136:143], v250\n" \
  "s_add_u32 m0, m0, 19968\n" \
  "s_add_u32 s70, s70, 593920\n" \
  "buffer_load_dwordx4 v11, s[60:63], s70 offen offset:0 sc1 lds\n" \
  "v_exp_f32 v146, v146\n" \
  "v_exp_f32 v147, v147\n" \
  "v_exp_f32 v148, v148\n" \
  "v_exp_f32 v149, v149\n" \
  "v_mfma_f32_32x32x16_bf16 v[160:175], v[224:227], v[240:243], v[160:175]\n" \
  "buffer_load_dwordx4 v12, s[60:63], s70 offen offset:1024 sc1 lds\n" \
  "v_exp_f32 v150, v150\n" \
  "v_exp_f32 v151, v151\n" \
  "v_exp_f32 v152, v152\n" \
  "v_exp_f32 v153, v153\n" \
  "v_smfmac_f32_32x32x32_bf16 v[208:223], v[232:235], v[144:151], v250\n" \
  "ds_read_b128 v[232:235], v253 offset:6272\n" \
  "s_mov_b64 exec, 0xfffffff\n" \
  "buffer_load_dwordx4 v13, s[60:63], s70 offen offset:2048 sc1 lds\n" \
  "s_mov_b64 exec, -1\n" \
  "v_exp_f32 v154, v154\n" \
  "v_exp_f32 v155, v155\n" \
  "v_exp_f32 v156, v156\n" \
  "v_exp_f32 v157, v157\n" \
  "v_mfma_f32_32x32x16_bf16 v[176:191], v[224:227], v[244:247], v[176:191]\n" \
  "ds_read_b64 v[224:225], v251 offset:9984\n" \
  "ds_read_b64 v[226:227], v251 offset:10240\n" \
  "v_exp_f32 v158, v158\n" \
  "v_exp_f32 v159, v159\n" \
  "s_waitcnt vmcnt(7)\n" \
  "s_barrier\n" \
  "v_exp_f32 v160, v160\n" \
  "v_exp_f32 v161, v161\n" \
  "v_smfmac_f32_32x32x32_bf16 v[208:223], v[236:239], v[152:159], v250\n" \
  "ds_read_b128 v[236:239], v253 offset:6880\n" \
  "s_setprio 3\n" \
  "v_exp_f32 v162, v162\n" \
  "v_exp_f32 v163, v163\n" \
  "v_exp_f32 v164, v164\n" \
  "v_exp_f32 v165, v165\n" \
  "v_mfma_f32_32x32x4_2b_bf16 v[128:159], v[228:229], v[248:249], 0\n" \
  "ds_read_b32 v228, v252 offset:19968\n" \
  "v_exp_f32 v166, v166\n" \
  "v_exp_f32 v167, v167\n" \
  "v_exp_f32 v168, v168\n" \
  "v_exp_f32 v169, v169\n" \
  "v_exp_f32 v170, v170\n" \
  "v_exp_f32 v171, v171\n" \
  "v_exp_f32 v172, v172\n" \
  "v_exp_f32 v173, v173\n" \
  "s_waitcnt lgkmcnt(1)\n" \
  "v_smfmac_f32_32x32x32_bf16 v[192:207], v[232:235], v[160:167], v250\n" \
  "s_add_u32 m0, m0, 19968\n" \
  "s_add_u32 s70, s70, 593920\n" \
  "buffer_load_dwordx4 v11, s[60:63], s70 offen offset:0 sc1 lds\n" \
  "v_exp_f32 v174, v174\n" \
  "v_exp_f32 v175, v175\n" \
  "v_exp_f32 v176, v176\n" \
  "v_exp_f32 v177, v177\n" \
  "v_smfmac_f32_32x32x32_bf16 v[192:207], v[236:239], v[168:175], v250\n" \
  "buffer_load_dwordx4 v12, s[60:63], s70 offen offset:1024 sc1 lds\n" \
  "v_exp_f32 v178, v178\n" \
  "v_exp_f32 v179, v179\n" \
  "v_exp_f32 v180, v180\n" \
  "v_exp_f32 v181, v181\n" \
  "v_mfma_f32_32x32x16_bf16 v[128:143], v[224:227], v[240:243], v[128:143]\n" \
  "s_mov_b64 exec, 0xfffffff\n" \
  "buffer_load_dwordx4 v13, s[60:63], s70 offen offset:2048 sc1 lds\n" \
  "s_mov_b64 exec, -1\n" \
  "v_exp_f32 v182, v182\n" \
  "v_exp_f32 v183, v183\n" \
  "v_exp_f32 v184, v184\n" \
  "v_exp_f32 v185, v185\n" \
  "v_smfmac_f32_32x32x32_bf16 v[208:223], v[232:235], v[176:183], v250\n" \
  "ds_read_b128 v[232:235], v253 offset:11264\n" \
  "s_add_u32 m0, m0, 19968\n" \
  "s_add_u32 s70, s70, 593920\n" \
  "buffer_load_dwordx4 v11, s[60:63], s70 offen offset:0 sc1 lds\n" \
  "v_exp_f32 v186, v186\n" \
  "v_exp_f32 v187, v187\n" \
  "v_exp_f32 v188, v188\n" \
  "v_exp_f32 v189, v189\n" \
  "v_mfma_f32_32x32x16_bf16 v[144:159], v[224:227], v[244:247], v[144:159]\n" \
  "ds_read_b64 v[224:225], v251 offset:14976\n" \
  "ds_read_b64 v[226:227], v251 offset:15232\n" \
  "v_exp_f32 v190, v190\n" \
  "v_exp_f32 v191, v191\n" \
  "v_exp_f32 v128, v128\n" \
  "v_exp_f32 v129, v129\n" \
  "v_smfmac_f32_32x32x32_bf16 v[208:223], v[236:239], v[184:191], v250\n" \
  "ds_read_b128 v[236:239], v253 offset:11872\n" \
  "s_setprio 2\n" \
  "v_exp_f32 v130, v130\n" \
  "v_exp_f32 v131, v131\n" \
  "v_exp_f32 v132, v132\n" \
  "v_exp_f32 v133, v133\n" \
  "v_mfma_f32_32x32x4_2b_bf16 v[160:191], v[230:231], v[248:249], 0\n" \
  "ds_read_b32 v230, v252 offset:24960\n" \
  "v_exp_f32 v134, v134\n" \
  "v_exp_f32 v135, v135\n" \
  "v_exp_f32 v136, v136\n" \
  "v_exp_f32 v137, v137\n" \
  "v_exp_f32 v138, v138\n" \
  "v_exp_f32 v139, v139\n" \
  "v_exp_f32 v140, v140\n" \
  "v_exp_f32 v141, v141\n" \
  "s_waitcnt lgkmcnt(1)\n" \
  "v_smfmac_f32_32x32x32_bf16 v[192:207], v[232:235], v[128:135], v250\n" \
  "buffer_load_dwordx4 v12, s[60:63], s70 offen offset:1024 sc1 lds\n" \
  "v_exp_f32 v142, v142\n" \
  "v_exp_f32 v143, v143\n" \
  "v_exp_f32 v144, v144\n" \
  "v_exp_f32 v145, v145\n" \
  "v_smfmac_f32_32x32x32_bf16 v[192:207], v[236:239], v[136:143], v250\n" \
  "s_mov_b64 exec, 0xfffffff\n" \
  "buffer_load_dwordx4 v13, s[60:63], s70 offen offset:2048 sc1 lds\n" \
  "s_mov_b64 exec, -1\n" \
  "v_exp_f32 v146, v146\n" \
  "v_exp_f32 v147, v147\n" \
  "v_exp_f32 v148, v148\n" \
  "v_exp_f32 v149, v149\n" \
  "v_mfma_f32_32x32x16_bf16 v[160:175], v[224:227], v[240:243], v[160:175]\n" \
  "s_add_u32 m0, m0, 19968\n" \
  "s_add_u32 s70, s70, 593920\n" \
  "buffer_load_dwordx4 v11, s[60:63], s70 offen offset:0 sc1 lds\n" \
  "v_exp_f32 v150, v150\n" \
  "v_exp_f32 v151, v151\n" \
  "v_exp_f32 v152, v152\n" \
  "v_exp_f32 v153, v153\n" \
  "v_smfmac_f32_32x32x32_bf16 v[208:223], v[232:235], v[144:151], v250\n" \
  "ds_read_b128 v[232:235], v253 offset:16256\n" \
  "buffer_load_dwordx4 v12, s[60:63], s70 offen offset:1024 sc1 lds\n" \
  "v_exp_f32 v154, v154\n" \
  "v_exp_f32 v155, v155\n" \
  "v_exp_f32 v156, v156\n" \
  "v_exp_f32 v157, v157\n" \
  "v_mfma_f32_32x32x16_bf16 v[176:191], v[224:227], v[244:247], v[176:191]\n" \
  "ds_read_b64 v[224:225], v251 offset:19968\n" \
  "ds_read_b64 v[226:227], v251 offset:20224\n" \
  "v_exp_f32 v158, v158\n" \
  "v_exp_f32 v159, v159\n" \
  "v_exp_f32 v160, v160\n" \
  "v_exp_f32 v161, v161\n" \
  "v_smfmac_f32_32x32x32_bf16 v[208:223], v[236:239], v[152:159], v250\n" \
  "ds_read_b128 v[236:239], v253 offset:16864\n" \
  "s_setprio 1\n" \
  "v_exp_f32 v162, v162\n" \
  "v_exp_f32 v163, v163\n" \
  "v_exp_f32 v164, v164\n" \
  "v_exp_f32 v165, v165\n" \
  "v_mfma_f32_32x32x4_2b_bf16 v[128:159], v[228:229], v[248:249], 0\n" \
  "ds_read_b32 v228, v252 offset:29952\n" \
  "v_exp_f32 v166, v166\n" \
  "v_exp_f32 v167, v167\n" \
  "v_exp_f32 v168, v168\n" \
  "v_exp_f32 v169, v169\n" \
  "v_exp_f32 v170, v170\n" \
  "v_exp_f32 v171, v171\n" \
  "v_exp_f32 v172, v172\n" \
  "v_exp_f32 v173, v173\n" \
  "s_waitcnt lgkmcnt(1)\n" \
  "v_smfmac_f32_32x32x32_bf16 v[192:207], v[232:235], v[160:167], v250\n" \
  "s_waitcnt vmcnt(11)\n" \
  "s_mov_b64 exec, 0xfffffff\n" \
  "buffer_load_dwordx4 v13, s[60:63], s70 offen offset:2048 sc1 lds\n" \
  "s_mov_b64 exec, -1\n" \
  "v_readlane_b32 s80, v22, 0\n" \
  "v_readlane_b32 s81, v22, 1\n" \
  "v_readlane_b32 s82, v22, 2\n" \
  "v_readlane_b32 s83, v22, 3\n" \
  "v_exp_f32 v174, v174\n" \
  "v_exp_f32 v175, v175\n" \
  "v_exp_f32 v176, v176\n" \
  "v_exp_f32 v177, v177\n" \
  "v_smfmac_f32_32x32x32_bf16 v[192:207], v[236:239], v[168:175], v250\n" \
  "s_add_u32 m0, m0, 19968\n" \
  "s_add_u32 s70, s70, 593920\n" \
  "buffer_load_dwordx4 v11, s[60:63], s70 offen offset:0 sc1 lds\n" \
  "v_readlane_b32 s84, v22, 4\n" \
  "v_readlane_b32 s85, v22, 5\n" \
  "v_readlane_b32 s86, v22, 6\n" \
  "v_readlane_b32 s87, v22, 7\n" \
  "v_exp_f32 v178, v178\n" \
  "v_exp_f32 v179, v179\n" \
  "v_exp_f32 v180, v180\n" \
  "v_exp_f32 v181, v181\n" \
  "v_mfma_f32_32x32x16_bf16 v[128:143], v[224:227], v[240:243], v[128:143]\n" \
  "buffer_load_dwordx4 v12, s[60:63], s70 offen offset:1024 sc1 lds\n" \
  "v_readlane_b32 s88, v22, 8\n" \
  "v_readlane_b32 s89, v22, 9\n" \
  "v_readlane_b32 s90, v22, 10\n" \
  "v_readlane_b32 s91, v22, 11\n" \
  "v_readfirstlane_b32 s75, v4\n" \
  "v_exp_f32 v182, v182\n" \
  "v_exp_f32 v183, v183\n" \
  "v_exp_f32 v184, v184\n" \
  "v_exp_f32 v185, v185\n" \
  "v_smfmac_f32_32x32x32_bf16 v[208:223], v[232:235], v[176:183], v250\n" \
  "ds_read_b128 v[232:235], v253 offset:21248\n" \
  "s_mov_b64 exec, 0xfffffff\n" \
  "buffer_load_dwordx4 v13, s[60:63], s70 offen offset:2048 sc1 lds\n" \
  "s_mov_b64 exec, -1\n" \
  "v_readlane_b32 s92, v22, 12\n" \
  "v_readlane_b32 s93, v22, 13\n" \
  "v_readlane_b32 s94, v22, 14\n" \
  "v_readlane_b32 s95, v22, 15\n" \
  "v_exp_f32 v186, v186\n" \
  "v_exp_f32 v187, v187\n" \
  "v_exp_f32 v188, v188\n" \
  "v_exp_f32 v189, v189\n" \
  "v_mfma_f32_32x32x16_bf16 v[144:159], v[224:227], v[244:247], v[144:159]\n" \
  "ds_read_b64 v[224:225], v251 offset:24960\n" \
  "ds_read_b64 v[226:227], v251 offset:25216\n" \
  "v_exp_f32 v190, v190\n" \
  "v_exp_f32 v191, v191\n" \
  "v_exp_f32 v128, v128\n" \
  "v_exp_f32 v129, v129\n" \
  "v_smfmac_f32_32x32x32_bf16 v[208:223], v[236:239], v[184:191], v250\n" \
  "ds_read_b128 v[236:239], v253 offset:21856\n" \
  "s_setprio 0\n" \
  "v_exp_f32 v130, v130\n" \
  "v_exp_f32 v131, v131\n" \
  "v_exp_f32 v132, v132\n" \
  "v_exp_f32 v133, v133\n" \
  "v_mfma_f32_32x32x4_2b_bf16 v[160:191], v[230:231], v[248:249], 0\n" \
  "ds_read_b32 v230, v252 offset:34944\n" \
  "v_exp_f32 v134, v134\n" \
  "v_exp_f32 v135, v135\n" \
  "v_exp_f32 v136, v136\n" \
  "v_exp_f32 v137, v137\n" \
  "v_exp_f32 v138, v138\n" \
  "v_exp_f32 v139, v139\n" \
  "v_exp_f32 v140, v140\n" \
  "v_exp_f32 v141, v141\n" \
  "s_waitcnt lgkmcnt(1)\n" \
  "v_smfmac_f32_32x32x32_bf16 v[192:207], v[232:235], v[128:135], v250\n" \
  "s_cmp_eq_u32 s83, s75\n" \
  "s_cselect_b32 s82, s82, 0\n" \
  "s_cselect_b64 s[80:81], s[80:81], s[60:61]\n" \
  "s_mov_b32 s83, 0x20000\n" \
  "buffer_load_dwordx4 v[48:51], v16, s[60:63], s79 offen offset:0 sc1\n" \
  "v_exp_f32 v142, v142\n" \
  "v_exp_f32 v143, v143\n" \
  "v_exp_f32 v144, v144\n" \
  "v_exp_f32 v145, v145\n" \
  "v_smfmac_f32_32x32x32_bf16 v[192:207], v[236:239], v[136:143], v250\n" \
  "s_cmp_eq_u32 s87, s75\n" \
  "s_cselect_b32 s86, s86, 0\n" \
  "s_cselect_b64 s[84:85], s[84:85], s[60:61]\n" \
  "s_mov_b32 s87, 0x20000\n" \
  "buffer_load_dwordx4 v[52:55], v16, s[60:63], s79 offen offset:1024 sc1\n" \
  "v_exp_f32 v146, v146\n" \
  "v_exp_f32 v147, v147\n" \
  "v_exp_f32 v148, v148\n" \
  "v_exp_f32 v149, v149\n" \
  "v_mfma_f32_32x32x16_bf16 v[160:175], v[224:227], v[240:243], v[160:175]\n" \
  "buffer_load_dwordx4 v[56:59], v16, s[60:63], s79 offen offset:2048 sc1\n" \
  "v_exp_f32 v150, v150\n" \
  "v_exp_f32 v151, v151\n" \
  "v_exp_f32 v152, v152\n" \
  "v_exp_f32 v153, v153\n" \
  "v_smfmac_f32_32x32x32_bf16 v[208:223], v[232:235], v[144:151], v250\n" \
  "ds_read_b128 v[232:235], v253 offset:26240\n" \
  "buffer_load_dwordx4 v[60:63], v16, s[60:63], s79 offen offset:3072 sc1\n" \
  "s_add_u32 s79, s79, 4096\n" \
  "v_exp_f32 v154, v154\n" \
  "v_exp_f32 v155, v155\n" \
  "v_exp_f32 v156, v156\n" \
  "v_exp_f32 v157, v157\n" \
  "v_mfma_f32_32x32x16_bf16 v[176:191], v[224:227], v[244:247], v[176:191]\n" \
  "ds_read_b64 v[224:225], v251 offset:29952\n" \
  "ds_read_b64 v[226:227], v251 offset:30208\n" \
  "v_exp_f32 v158, v158\n" \
  "v_exp_f32 v159, v159\n" \
  "s_waitcnt vmcnt(13)\n" \
  "s_barrier\n" \
  "v_exp_f32 v160, v160\n" \
  "v_exp_f32 v161, v161\n" \
  "v_smfmac_f32_32x32x32_bf16 v[208:223], v[236:239], v[152:159], v250\n" \
  "ds_read_b128 v[236:239], v253 offset:26848\n" \
  "s_setprio 3\n" \
  "v_exp_f32 v162, v162\n" \
  "v_exp_f32 v163, v163\n" \
  "v_exp_f32 v164, v164\n" \
  "v_exp_f32 v165, v165\n" \
  "v_mfma_f32_32x32x4_2b_bf16 v[128:159], v[228:229], v[248:249], 0\n" \
  "ds_read_b32 v228, v252 offset:39936\n" \
  "v_exp_f32 v166, v166\n" \
  "v_exp_f32 v167, v167\n" \
  "v_exp_f32 v168, v168\n" \
  "v_exp_f32 v169, v169\n" \
  "v_exp_f32 v170, v170\n" \
  "v_exp_f32 v171, v171\n" \
  "v_exp_f32 v172, v172\n" \
  "v_exp_f32 v173, v173\n" \
  "s_waitcnt lgkmcnt(1)\n" \
  "v_smfmac_f32_32x32x32_bf16 v[192:207], v[232:235], v[160:167], v250\n" \
  "buffer_load_dwordx4 v[64:67], v16, s[60:63], s79 offen offset:0 sc1\n" \
  "v_exp_f32 v174, v174\n" \
  "v_exp_f32 v175, v175\n" \
  "v_exp_f32 v176, v176\n" \
  "v_exp_f32 v177, v177\n" \
  "v_smfmac_f32_32x32x32_bf16 v[192:207], v[236:239], v[168:175], v250\n" \
  "buffer_load_dwordx4 v[68:71], v16, s[60:63], s79 offen offset:1024 sc1\n" \
  "v_exp_f32 v178, v178\n" \
  "v_exp_f32 v179, v179\n" \
  "v_exp_f32 v180, v180\n" \
  "v_exp_f32 v181, v181\n" \
  "v_mfma_f32_32x32x16_bf16 v[128:143], v[224:227], v[240:243], v[128:143]\n" \
  "buffer_load_dwordx4 v[72:75], v16, s[60:63], s79 offen offset:2048 sc1\n" \
  "v_exp_f32 v182, v182\n" \
  "v_exp_f32 v183, v183\n" \
  "v_exp_f32 v184, v184\n" \
  "v_exp_f32 v185, v185\n" \
  "v_smfmac_f32_32x32x32_bf16 v[208:223], v[232:235], v[176:183], v250\n" \
  "ds_read_b128 v[232:235], v253 offset:31232\n" \
  "buffer_load_dwordx4 v[76:79], v16, s[60:63], s79 offen offset:3072 sc1\n" \
  "s_add_u32 s79, s79, 4096\n" \
  "v_exp_f32 v186, v186\n" \
  "v_exp_f32 v187, v187\n" \
  "v_exp_f32 v188, v188\n" \
  "v_exp_f32 v189, v189\n" \
  "v_mfma_f32_32x32x16_bf16 v[144:159], v[224:227], v[244:247], v[144:159]\n" \
  "ds_read_b64 v[224:225], v251 offset:34944\n" \
  "ds_read_b64 v[226:227], v251 offset:35200\n" \
  "v_exp_f32 v190, v190\n" \
  "v_exp_f32 v191, v191\n" \
  "v_exp_f32 v128, v128\n" \
  "v_exp_f32 v129, v129\n" \
  "v_smfmac_f32_32x32x32_bf16 v[208:223], v[236:239], v[184:191], v250\n" \
  "ds_read_b128 v[236:239], v253 offset:31840\n" \
  "v_exp_f32 v130, v130\n" \
  "v_exp_f32 v131, v131\n" \
  "v_exp_f32 v132, v132\n" \
  "v_exp_f32 v133, v133\n" \
  "v_mfma_f32_32x32x4_2b_bf16 v[160:191], v[230:231], v[248:249], 0\n" \
  "ds_read_b32 v230, v252 offset:44928\n" \
  "v_exp_f32 v134, v134\n" \
  "v_exp_f32 v135, v135\n" \
  "v_exp_f32 v136, v136\n" \
  "v_exp_f32 v137, v137\n" \
  "v_exp_f32 v138, v138\n" \
  "v_exp_f32 v139, v139\n" \
  "v_exp_f32 v140, v140\n" \
  "v_exp_f32 v141, v141\n" \
  "s_waitcnt lgkmcnt(1)\n" \
  "v_smfmac_f32_32x32x32_bf16 v[192:207], v[232:235], v[128:135], v250\n" \
  "buffer_load_dwordx4 v[80:83], v16, s[60:63], s79 offen offset:0 sc1\n" \
  "v_exp_f32 v142, v142\n" \
  "v_exp_f32 v143, v143\n" \
  "v_exp_f32 v144, v144\n" \
  "v_exp_f32 v145, v145\n" \
  "v_smfmac_f32_32x32x32_bf16 v[192:207], v[236:239], v[136:143], v250\n" \
  "buffer_load_dwordx4 v[84:87], v16, s[60:63], s67 offen offset:0 sc1\n" \
  "v_exp_f32 v146, v146\n" \
  "v_exp_f32 v147, v147\n" \
  "v_exp_f32 v148, v148\n" \
  "v_exp_f32 v149, v149\n" \
  "v_mfma_f32_32x32x16_bf16 v[160:175], v[224:227], v[240:243], v[160:175]\n" \
  "buffer_load_dwordx4 v[88:91], v16, s[60:63], s67 offen offset:1024 sc1\n" \
  "v_exp_f32 v150, v150\n" \
  "v_exp_f32 v151, v151\n" \
  "v_exp_f32 v152, v152\n" \
  "v_exp_f32 v153, v153\n" \
  "v_smfmac_f32_32x32x32_bf16 v[208:223], v[232:235], v[144:151], v250\n" \
  "ds_read_b128 v[232:235], v253 offset:36224\n" \
  "buffer_load_dwordx4 v[92:95], v16, s[60:63], s67 offen offset:2048 sc1\n" \
  "s_add_u32 s67, s67, 7168\n" \
  "v_exp_f32 v154, v154\n" \
  "v_exp_f32 v155, v155\n" \
  "v_exp_f32 v156, v156\n" \
  "v_exp_f32 v157, v157\n" \
  "v_mfma_f32_32x32x16_bf16 v[176:191], v[224:227], v[244:247], v[176:191]\n" \
  "ds_read_b64 v[224:225], v251 offset:39936\n" \
  "ds_read_b64 v[226:227], v251 offset:40192\n" \
  "v_exp_f32 v158, v158\n" \
  "v_exp_f32 v159, v159\n" \
  "v_exp_f32 v160, v160\n" \
  "v_exp_f32 v161, v161\n" \
  "v_smfmac_f32_32x32x32_bf16 v[208:223], v[236:239], v[152:159], v250\n" \
  "ds_read_b128 v[236:239], v253 offset:36832\n" \
  "v_exp_f32 v162, v162\n" \
  "v_exp_f32 v163, v163\n" \
  "v_exp_f32 v164, v164\n" \
  "v_exp_f32 v165, v165\n" \
  "v_mfma_f32_32x32x4_2b_bf16 v[128:159], v[228:229], v[248:249], 0\n" \
  "ds_read_b32 v228, v252 offset:49920\n" \
  "v_exp_f32 v166, v166\n" \
  "v_exp_f32 v167, v167\n" \
  "v_exp_f32 v168, v168\n" \
  "v_exp_f32 v169, v169\n" \
  "v_exp_f32 v170, v170\n" \
  "v_exp_f32 v171, v171\n" \
  "v_exp_f32 v172, v172\n" \
  "v_exp_f32 v173, v173\n" \
  "s_waitcnt lgkmcnt(1)\n" \
  "v_smfmac_f32_32x32x32_bf16 v[192:207], v[232:235], v[160:167], v250\n" \
  "buffer_load_dwordx4 v[8:11], v16, s[60:63], s67 offen offset:0 sc1\n" \
  "v_exp_f32 v174, v174\n" \
  "v_exp_f32 v175, v175\n" \
  "v_exp_f32 v176, v176\n" \
  "v_exp_f32 v177, v177\n" \
  "v_smfmac_f32_32x32x32_bf16 v[192:207], v[236:239], v[168:175], v250\n" \
  "buffer_load_dwordx4 v[12:15], v16, s[60:63], s67 offen offset:1024 sc1\n" \
  "s_add_u32 s67, s67, 4294963200\n" \
  "v_exp_f32 v178, v178\n" \
  "v_exp_f32 v179, v179\n" \
  "v_exp_f32 v180, v180\n" \
  "v_exp_f32 v181, v181\n" \
  "v_mfma_f32_32x32x16_bf16 v[128:143], v[224:227], v[240:243], v[128:143]\n" \
  "global_load_dwordx2 v[4:5], v29, s[56:57]\n" \
  "v_exp_f32 v182, v182\n" \
  "v_exp_f32 v183, v183\n" \
  "v_exp_f32 v184, v184\n" \
  "v_exp_f32 v185, v185\n" \
  "v_smfmac_f32_32x32x32_bf16 v[208:223], v[232:235], v[176:183], v250\n" \
  "ds_read_b128 v[232:235], v253 offset:41216\n" \
  "v_add_u32 v254, 18432, v29\n" \
  "global_load_dwordx2 v[6:7], v254, s[56:57]\n" \
  "v_exp_f32 v186, v186\n" \
  "v_exp_f32 v187, v187\n" \
  "v_exp_f32 v188, v188\n" \
  "v_exp_f32 v189, v189\n" \
  "v_mfma_f32_32x32x16_bf16 v[144:159], v[224:227], v[244:247], v[144:159]\n" \
  "ds_read_b64 v[224:225], v251 offset:44928\n" \
  "ds_read_b64 v[226:227], v251 offset:45184\n" \
  "v_exp_f32 v190, v190\n" \
  "v_exp_f32 v191, v191\n" \
  "v_exp_f32 v128, v128\n" \
  "v_exp_f32 v129, v129\n" \
  "v_smfmac_f32_32x32x32_bf16 v[208:223], v[236:239], v[184:191], v250\n" \
  "ds_read_b128 v[236:239], v253 offset:41824\n" \
  "v_exp_f32 v130, v130\n" \
  "v_exp_f32 v131, v131\n" \
  "v_exp_f32 v132, v132\n" \
  "v_exp_f32 v133, v133\n" \
  "v_mfma_f32_32x32x4_2b_bf16 v[160:191], v[230:231], v[248:249], 0\n" \
  "ds_read_b32 v230, v252 offset:54912\n" \
  "v_exp_f32 v134, v134\n" \
  "v_exp_f32 v135, v135\n" \
  "v_exp_f32 v136, v136\n" \
  "v_exp_f32 v137, v137\n" \
  "v_exp_f32 v138, v138\n" \
  "v_exp_f32 v139, v139\n" \
  "v_exp_f32 v140, v140\n" \
  "v_exp_f32 v141, v141\n" \
  "s_waitcnt lgkmcnt(1)\n" \
  "v_smfmac_f32_32x32x32_bf16 v[192:207], v[232:235], v[128:135], v250\n" \
  "global_load_dwordx2 v[18:19], v28, s[56:57]\n" \
  "v_exp_f32 v142, v142\n" \
  "v_exp_f32 v143, v143\n" \
  "v_exp_f32 v144, v144\n" \
  "v_exp_f32 v145, v145\n" \
  "v_smfmac_f32_32x32x32_bf16 v[192:207], v[236:239], v[136:143], v250\n" \
  "v_add_u32 v254, 18432, v28\n" \
  "global_load_dwordx2 v[28:29], v254, s[56:57]\n" \
  "v_exp_f32 v146, v146\n" \
  "v_exp_f32 v147, v147\n" \
  "v_exp_f32 v148, v148\n" \
  "v_exp_f32 v149, v149\n" \
  "v_mfma_f32_32x32x16_bf16 v[160:175], v[224:227], v[240:243], v[160:175]\n" \
  "s_getpc_b64 vcc\n" \
  "90:\n" \
  "v_min_u32 v21, .Lffn_code_end-90b, v21\n" \
  "v_exp_f32 v150, v150\n" \
  "v_exp_f32 v151, v151\n" \
  "v_exp_f32 v152, v152\n" \
  "v_exp_f32 v153, v153\n" \
  "v_smfmac_f32_32x32x32_bf16 v[208:223], v[232:235], v[144:151], v250\n" \
  "ds_read_b128 v[232:235], v253 offset:46208\n" \
  "v_lshl_add_u32 v17, v16, 22, s59\n" \
  "global_load_dword v21, v21, vcc\n" \
  "v_exp_f32 v154, v154\n" \
  "v_exp_f32 v155, v155\n" \
  "v_exp_f32 v156, v156\n" \
  "v_exp_f32 v157, v157\n" \
  "v_mfma_f32_32x32x16_bf16 v[176:191], v[224:227], v[244:247], v[176:191]\n" \
  "ds_read_b64 v[224:225], v251 offset:49920\n" \
  "ds_read_b64 v[226:227], v251 offset:50176\n" \
  "v_exp_f32 v158, v158\n" \
  "v_exp_f32 v159, v159\n" \
  "v_exp_f32 v160, v160\n" \
  "v_exp_f32 v161, v161\n" \
  "v_smfmac_f32_32x32x32_bf16 v[208:223], v[236:239], v[152:159], v250\n" \
  "ds_read_b128 v[236:239], v253 offset:46816\n" \
  "v_exp_f32 v162, v162\n" \
  "v_exp_f32 v163, v163\n" \
  "v_exp_f32 v164, v164\n" \
  "v_exp_f32 v165, v165\n" \
  "v_mfma_f32_32x32x4_2b_bf16 v[128:159], v[228:229], v[248:249], 0\n" \
  "ds_read_b32 v228, v252 offset:59904\n" \
  "v_exp_f32 v166, v166\n" \
  "v_exp_f32 v167, v167\n" \
  "v_exp_f32 v168, v168\n" \
  "v_exp_f32 v169, v169\n" \
  "v_exp_f32 v170, v170\n" \
  "v_exp_f32 v171, v171\n" \
  "v_exp_f32 v172, v172\n" \
  "v_exp_f32 v173, v173\n" \
  "s_waitcnt lgkmcnt(1)\n" \
  "v_smfmac_f32_32x32x32_bf16 v[192:207], v[232:235], v[160:167], v250\n" \
  "global_load_dword v20, v30, s[68:69] sc1\n" \
  "global_load_dword v21, v30, s[68:69] offset:64 sc1\n" \
  "v_exp_f32 v174, v174\n" \
  "v_exp_f32 v175, v175\n" \
  "v_exp_f32 v176, v176\n" \
  "v_exp_f32 v177, v177\n" \
  "v_smfmac_f32_32x32x32_bf16 v[192:207], v[236:239], v[168:175], v250\n" \
  "v_mov_b64 v[32:33], -1\n" \
  "v_mov_b64 v[34:35], -1\n" \
  "buffer_store_dwordx4 v[32:35], v16, s[60:63], s96 offen sc1\n" \
  "v_exp_f32 v178, v178\n" \
  "v_exp_f32 v179, v179\n" \
  "v_exp_f32 v180, v180\n" \
  "v_exp_f32 v181, v181\n" \
  "v_mfma_f32_32x32x16_bf16 v[128:143], v[224:227], v[240:243], v[128:143]\n" \
  "buffer_store_dwordx4 v[32:35], v16, s[60:63], s97 offen sc1\n" \
  "v_exp_f32 v182, v182\n" \
  "v_exp_f32 v183, v183\n" \
  "v_exp_f32 v184, v184\n" \
  "v_exp_f32 v185, v185\n" \
  "v_smfmac_f32_32x32x32_bf16 v[208:223], v[232:235], v[176:183], v250\n" \
  "ds_read_b128 v[232:235], v253 offset:51200\n" \
  "v_exp_f32 v186, v186\n" \
  "v_exp_f32 v187, v187\n" \
  "v_exp_f32 v188, v188\n" \
  "v_exp_f32 v189, v189\n" \
  "v_mfma_f32_32x32x16_bf16 v[144:159], v[224:227], v[244:247], v[144:159]\n" \
  "ds_read_b64 v[224:225], v251 offset:54912\n" \
  "ds_read_b64 v[226:227], v251 offset:55168\n" \
  "v_add_u32 v251, 49920, v251\n" \
  "v_add_u32 v252, 49920, v252\n" \
  "v_add_u32 v253, 49920, v253\n" \
  "v_exp_f32 v190, v190\n" \
  "v_exp_f32 v191, v191\n" \
  "v_exp_f32 v128, v128\n" \
  "v_exp_f32 v129, v129\n" \
  "v_smfmac_f32_32x32x32_bf16 v[208:223], v[236:239], v[184:191], v250\n" \
  "ds_read_b128 v[236:239], v253 offset:1888\n" \
  "v_exp_f32 v130, v130\n" \
  "v_exp_f32 v131, v131\n" \
  "v_exp_f32 v132, v132\n" \
  "v_exp_f32 v133, v133\n" \
  "v_mfma_f32_32x32x4_2b_bf16 v[160:191], v[230:231], v[248:249], 0\n" \
  "ds_read_b32 v230, v252 offset:14976\n" \
  "v_exp_f32 v134, v134\n" \
  "v_exp_f32 v135, v135\n" \
  "v_exp_f32 v136, v136\n" \
  "v_exp_f32 v137, v137\n" \
  "v_exp_f32 v138, v138\n" \
  "v_exp_f32 v139, v139\n" \
  "v_exp_f32 v140, v140\n" \
  "v_exp_f32 v141, v141\n" \
  "s_waitcnt lgkmcnt(1)\n" \
  "v_smfmac_f32_32x32x32_bf16 v[192:207], v[232:235], v[128:135], v250\n" \
  "buffer_load_dwordx4 v[96:99], v24, s[80:83], 0 offen nt\n" \
  "v_exp_f32 v142, v142\n" \
  "v_exp_f32 v143, v143\n" \
  "v_exp_f32 v144, v144\n" \
  "v_exp_f32 v145, v145\n" \
  "v_smfmac_f32_32x32x32_bf16 v[192:207], v[236:239], v[136:143], v250\n" \
  "v_exp_f32 v146, v146\n" \
  "v_exp_f32 v147, v147\n" \
  "v_exp_f32 v148, v148\n" \
  "v_exp_f32 v149, v149\n" \
  "v_mfma_f32_32x32x16_bf16 v[160:175], v[224:227], v[240:243], v[160:175]\n" \
  "s_mov_b32 s75, s76\n" \
  "buffer_load_dwordx4 v[100:103], v24, s[80:83], s75 offen nt\n" \
  "v_exp_f32 v150, v150\n" \
  "v_exp_f32 v151, v151\n" \
  "v_exp_f32 v152, v152\n" \
  "v_exp_f32 v153, v153\n" \
  "v_smfmac_f32_32x32x32_bf16 v[208:223], v[232:235], v[144:151], v250\n" \
  "ds_read_b128 v[232:235], v253 offset:6272\n" \
  "v_exp_f32 v154, v154\n" \
  "v_exp_f32 v155, v155\n" \
  "v_exp_f32 v156, v156\n" \
  "v_exp_f32 v157, v157\n" \
  "v_mfma_f32_32x32x16_bf16 v[176:191], v[224:227], v[244:247], v[176:191]\n" \
  "ds_read_b64 v[224:225], v251 offset:9984\n" \
  "ds_read_b64 v[226:227], v251 offset:10240\n" \
  "v_exp_f32 v158, v158\n" \
  "v_exp_f32 v159, v159\n" \
  "v_exp_f32 v160, v160\n" \
  "v_exp_f32 v161, v161\n" \
  "v_smfmac_f32_32x32x32_bf16 v[208:223], v[236:239], v[152:159], v250\n" \
  "ds_read_b128 v[236:239], v253 offset:6880\n" \
  "v_exp_f32 v162, v162\n" \
  "v_exp_f32 v163, v163\n" \
  "v_exp_f32 v164, v164\n" \
  "v_exp_f32 v165, v165\n" \
  "v_mfma_f32_32x32x4_2b_bf16 v[128:159], v[228:229], v[248:249], 0\n" \
  "ds_read_b32 v228, v252 offset:19968\n" \
  "v_exp_f32 v166, v166\n" \
  "v_exp_f32 v167, v167\n" \
  "v_exp_f32 v168, v168\n" \
  "v_exp_f32 v169, v169\n" \
  "v_exp_f32 v170, v170\n" \
  "v_exp_f32 v171, v171\n" \
  "v_exp_f32 v172, v172\n" \
  "v_exp_f32 v173, v173\n" \
  "s_waitcnt lgkmcnt(1)\n" \
  "v_smfmac_f32_32x32x32_bf16 v[192:207], v[232:235], v[160:167], v250\n" \
  "s_add_u32 s75, s75, s76\n" \
  "buffer_load_dwordx4 v[104:107], v24, s[80:83], s75 offen nt\n" \
  "v_exp_f32 v174, v174\n" \
  "v_exp_f32 v175, v175\n" \
  "v_exp_f32 v176, v176\n" \
  "v_exp_f32 v177, v177\n" \
  "v_smfmac_f32_32x32x32_bf16 v[192:207], v[236:239], v[168:175], v250\n" \
  "v_exp_f32 v178, v178\n" \
  "v_exp_f32 v179, v179\n" \
  "v_exp_f32 v180, v180\n" \
  "v_exp_f32 v181, v181\n" \
  "v_mfma_f32_32x32x16_bf16 v[128:143], v[224:227], v[240:243], v[128:143]\n" \
  "s_add_u32 s75, s75, s76\n" \
  "buffer_load_dwordx4 v[108:111], v24, s[80:83], s75 offen nt\n" \
  "v_exp_f32 v182, v182\n" \
  "v_exp_f32 v183, v183\n" \
  "v_exp_f32 v184, v184\n" \
  "v_exp_f32 v185, v185\n" \
  "v_smfmac_f32_32x32x32_bf16 v[208:223], v[232:235], v[176:183], v250\n" \
  "ds_read_b128 v[232:235], v253 offset:11264\n" \
  "v_exp_f32 v186, v186\n" \
  "v_exp_f32 v187, v187\n" \
  "v_exp_f32 v188, v188\n" \
  "v_exp_f32 v189, v189\n" \
  "v_mfma_f32_32x32x16_bf16 v[144:159], v[224:227], v[244:247], v[144:159]\n" \
  "ds_read_b64 v[224:225], v251 offset:14976\n" \
  "ds_read_b64 v[226:227], v251 offset:15232\n" \
  "v_exp_f32 v190, v190\n" \
  "v_exp_f32 v191, v191\n" \
  "v_exp_f32 v128, v128\n" \
  "v_exp_f32 v129, v129\n" \
  "v_smfmac_f32_32x32x32_bf16 v[208:223], v[236:239], v[184:191], v250\n" \
  "ds_read_b128 v[236:239], v253 offset:11872\n" \
  "s_setprio 2\n" \
  "v_exp_f32 v130, v130\n" \
  "v_exp_f32 v131, v131\n" \
  "v_exp_f32 v132, v132\n" \
  "v_exp_f32 v133, v133\n" \
  "v_mfma_f32_32x32x4_2b_bf16 v[160:191], v[230:231], v[248:249], 0\n" \
  "ds_read_b32 v230, v252 offset:24960\n" \
  "v_exp_f32 v134, v134\n" \
  "v_exp_f32 v135, v135\n" \
  "v_exp_f32 v136, v136\n" \
  "v_exp_f32 v137, v137\n" \
  "v_exp_f32 v138, v138\n" \
  "v_exp_f32 v139, v139\n" \
  "v_exp_f32 v140, v140\n" \
  "v_exp_f32 v141, v141\n" \
  "s_waitcnt lgkmcnt(1)\n" \
  "v_smfmac_f32_32x32x32_bf16 v[192:207], v[232:235], v[128:135], v250\n" \
  "buffer_load_dwordx4 v[112:115], v25, s[84:87], 0 offen nt\n" \
  "v_exp_f32 v142, v142\n" \
  "v_exp_f32 v143, v143\n" \
  "v_exp_f32 v144, v144\n" \
  "v_exp_f32 v145, v145\n" \
  "v_smfmac_f32_32x32x32_bf16 v[192:207], v[236:239], v[136:143], v250\n" \
  "v_exp_f32 v146, v146\n" \
  "v_exp_f32 v147, v147\n" \
  "v_exp_f32 v148, v148\n" \
  "v_exp_f32 v149, v149\n" \
  "v_mfma_f32_32x32x16_bf16 v[160:175], v[224:227], v[240:243], v[160:175]\n" \
  "s_mov_b32 s75, s77\n" \
  "buffer_load_dwordx4 v[116:119], v25, s[84:87], s75 offen nt\n" \
  "v_exp_f32 v150, v150\n" \
  "v_exp_f32 v151, v151\n" \
  "v_exp_f32 v152, v152\n" \
  "v_exp_f32 v153, v153\n" \
  "v_smfmac_f32_32x32x32_bf16 v[208:223], v[232:235], v[144:151], v250\n" \
  "ds_read_b128 v[232:235], v253 offset:16256\n" \
  "v_exp_f32 v154, v154\n" \
  "v_exp_f32 v155, v155\n" \
  "v_exp_f32 v156, v156\n" \
  "v_exp_f32 v157, v157\n" \
  "v_mfma_f32_32x32x16_bf16 v[176:191], v[224:227], v[244:247], v[176:191]\n" \
  "ds_read_b64 v[224:225], v251 offset:19968\n" \
  "ds_read_b64 v[226:227], v251 offset:20224\n" \
  "v_exp_f32 v158, v158\n" \
  "v_exp_f32 v159, v159\n" \
  "v_exp_f32 v160, v160\n" \
  "v_exp_f32 v161, v161\n" \
  "v_smfmac_f32_32x32x32_bf16 v[208:223], v[236:239], v[152:159], v250\n" \
  "ds_read_b128 v[236:239], v253 offset:16864\n" \
  "v_exp_f32 v162, v162\n" \
  "v_exp_f32 v163, v163\n" \
  "v_exp_f32 v164, v164\n" \
  "v_exp_f32 v165, v165\n" \
  "v_mfma_f32_32x32x4_2b_bf16 v[128:159], v[228:229], v[248:249], 0\n" \
  "ds_read_b32 v228, v252 offset:29952\n" \
  "v_exp_f32 v166, v166\n" \
  "v_exp_f32 v167, v167\n" \
  "v_exp_f32 v168, v168\n" \
  "v_exp_f32 v169, v169\n" \
  "v_exp_f32 v170, v170\n" \
  "v_exp_f32 v171, v171\n" \
  "v_exp_f32 v172, v172\n" \
  "v_exp_f32 v173, v173\n" \
  "s_waitcnt lgkmcnt(1)\n" \
  "v_smfmac_f32_32x32x32_bf16 v[192:207], v[232:235], v[160:167], v250\n" \
  "s_add_u32 s75, s75, s77\n" \
  "buffer_load_dwordx4 v[120:123], v25, s[84:87], s75 offen nt\n" \
  "v_exp_f32 v174, v174\n" \
  "v_exp_f32 v175, v175\n" \
  "v_exp_f32 v176, v176\n" \
  "v_exp_f32 v177, v177\n" \
  "v_smfmac_f32_32x32x32_bf16 v[192:207], v[236:239], v[168:175], v250\n" \
  "v_exp_f32 v178, v178\n" \
  "v_exp_f32 v179, v179\n" \
  "v_exp_f32 v180, v180\n" \
  "v_exp_f32 v181, v181\n" \
  "v_mfma_f32_32x32x16_bf16 v[128:143], v[224:227], v[240:243], v[128:143]\n" \
  "s_add_u32 s75, s75, s77\n" \
  "buffer_load_dwordx4 v[124:127], v25, s[84:87], s75 offen nt\n" \
  "v_exp_f32 v182, v182\n" \
  "v_exp_f32 v183, v183\n" \
  "v_exp_f32 v184, v184\n" \
  "v_exp_f32 v185, v185\n" \
  "v_smfmac_f32_32x32x32_bf16 v[208:223], v[232:235], v[176:183], v250\n" \
  "ds_read_b128 v[232:235], v253 offset:21248\n" \
  "v_exp_f32 v186, v186\n" \
  "v_exp_f32 v187, v187\n" \
  "v_exp_f32 v188, v188\n" \
  "v_exp_f32 v189, v189\n" \
  "v_mfma_f32_32x32x16_bf16 v[144:159], v[224:227], v[244:247], v[144:159]\n" \
  "ds_read_b64 v[224:225], v251 offset:24960\n" \
  "ds_read_b64 v[226:227], v251 offset:25216\n" \
  "v_exp_f32 v190, v190\n" \
  "v_exp_f32 v191, v191\n" \
  "v_exp_f32 v128, v128\n" \
  "v_exp_f32 v129, v129\n" \
  "v_smfmac_f32_32x32x32_bf16 v[208:223], v[236:239], v[184:191], v250\n" \
  "ds_read_b128 v[236:239], v253 offset:21856\n" \
  "s_setprio 1\n" \
  "v_exp_f32 v130, v130\n" \
  "v_exp_f32 v131, v131\n" \
  "v_exp_f32 v132, v132\n" \
  "v_exp_f32 v133, v133\n" \
  "v_mfma_f32_32x32x4_2b_bf16 v[160:191], v[230:231], v[248:249], 0\n" \
  "ds_read_b32 v230, v252 offset:34944\n" \
  "v_exp_f32 v134, v134\n" \
  "v_exp_f32 v135, v135\n" \
  "v_exp_f32 v136, v136\n" \
  "v_exp_f32 v137, v137\n" \
  "v_exp_f32 v138, v138\n" \
  "v_exp_f32 v139, v139\n" \
  "v_exp_f32 v140, v140\n" \
  "v_exp_f32 v141, v141\n" \
  "s_waitcnt lgkmcnt(1)\n" \
  "v_smfmac_f32_32x32x32_bf16 v[192:207], v[232:235], v[128:135], v250\n" \
  "v_exp_f32 v142, v142\n" \
  "v_exp_f32 v143, v143\n" \
  "v_exp_f32 v144, v144\n" \
  "v_exp_f32 v145, v145\n" \
  "v_smfmac_f32_32x32x32_bf16 v[192:207], v[236:239], v[136:143], v250\n" \
  "v_exp_f32 v146, v146\n" \
  "v_exp_f32 v147, v147\n" \
  "v_exp_f32 v148, v148\n" \
  "v_exp_f32 v149, v149\n" \
  "v_mfma_f32_32x32x16_bf16 v[160:175], v[224:227], v[240:243], v[160:175]\n" \
  "v_exp_f32 v150, v150\n" \
  "v_exp_f32 v151, v151\n" \
  "v_exp_f32 v152, v152\n" \
  "v_exp_f32 v153, v153\n" \
  "v_smfmac_f32_32x32x32_bf16 v[208:223], v[232:235], v[144:151], v250\n" \
  "ds_read_b128 v[232:235], v253 offset:26240\n" \
  "v_exp_f32 v154, v154\n" \
  "v_exp_f32 v155, v155\n" \
  "v_exp_f32 v156, v156\n" \
  "v_exp_f32 v157, v157\n" \
  "v_mfma_f32_32x32x16_bf16 v[176:191], v[224:227], v[244:247], v[176:191]\n" \
  "ds_read_b64 v[224:225], v251 offset:29952\n" \
  "ds_read_b64 v[226:227], v251 offset:30208\n" \
  "v_exp_f32 v158, v158\n" \
  "v_exp_f32 v159, v159\n" \
  "v_exp_f32 v160, v160\n" \
  "v_exp_f32 v161, v161\n" \
  "v_smfmac_f32_32x32x32_bf16 v[208:223], v[236:239], v[152:159], v250\n" \
  "ds_read_b128 v[236:239], v253 offset:26848\n" \
  "s_setprio 0\n" \
  "v_exp_f32 v162, v162\n" \
  "v_exp_f32 v163, v163\n" \
  "v_exp_f32 v164, v164\n" \
  "v_exp_f32 v165, v165\n" \
  "v_mfma_f32_32x32x4_2b_bf16 v[128:159], v[228:229], v[248:249], 0\n" \
  "ds_read_b32 v228, v252 offset:39936\n" \
  "v_exp_f32 v166, v166\n" \
  "v_exp_f32 v167, v167\n" \
  "v_exp_f32 v168, v168\n" \
  "v_exp_f32 v169, v169\n" \
  "v_exp_f32 v170, v170\n" \
  "v_exp_f32 v171, v171\n" \
  "v_exp_f32 v172, v172\n" \
  "v_exp_f32 v173, v173\n" \
  "s_waitcnt lgkmcnt(1)\n" \
  "v_smfmac_f32_32x32x32_bf16 v[192:207], v[232:235], v[160:167], v250\n" \
  "v_exp_f32 v174, v174\n" \
  "v_exp_f32 v175, v175\n" \
  "v_exp_f32 v176, v176\n" \
  "v_exp_f32 v177, v177\n" \
  "v_smfmac_f32_32x32x32_bf16 v[192:207], v[236:239], v[168:175], v250\n" \
  "v_exp_f32 v178, v178\n" \
  "v_exp_f32 v179, v179\n" \
  "v_exp_f32 v180, v180\n" \
  "v_exp_f32 v181, v181\n" \
  "v_mfma_f32_32x32x16_bf16 v[128:143], v[224:227], v[240:243], v[128:143]\n" \
  "v_exp_f32 v182, v182\n" \
  "v_exp_f32 v183, v183\n" \
  "v_exp_f32 v184, v184\n" \
  "v_exp_f32 v185, v185\n" \
  "v_smfmac_f32_32x32x32_bf16 v[208:223], v[232:235], v[176:183], v250\n" \
  "ds_read_b128 v[232:235], v253 offset:31232\n" \
  "v_exp_f32 v186, v186\n" \
  "v_exp_f32 v187, v187\n" \
  "v_exp_f32 v188, v188\n" \
  "v_exp_f32 v189, v189\n" \
  "v_mfma_f32_32x32x16_bf16 v[144:159], v[224:227], v[244:247], v[144:159]\n" \
  "ds_read_b64 v[224:225], v251 offset:34944\n" \
  "ds_read_b64 v[226:227], v251 offset:35200\n" \
  "v_exp_f32 v190, v190\n" \
  "v_exp_f32 v191, v191\n" \
  "s_waitcnt vmcnt(8)\n" \
  "s_barrier\n" \
  "buffer_store_dword v250, v17, s[60:63], s98 offen sc1\n" \
  "v_exp_f32 v128, v128\n" \
  "v_exp_f32 v129, v129\n" \
  "v_smfmac_f32_32x32x32_bf16 v[208:223], v[236:239], v[184:191], v250\n" \
  "ds_read_b128 v[236:239], v253 offset:31840\n" \
  "s_setprio 3\n" \
  "v_exp_f32 v130, v130\n" \
  "v_exp_f32 v131, v131\n" \
  "v_exp_f32 v132, v132\n" \
  "v_exp_f32 v133, v133\n" \
  "v_mfma_f32_32x32x4_2b_bf16 v[160:191], v[230:231], v[248:249], 0\n" \
  "ds_read_b32 v230, v252 offset:44928\n" \
  "v_exp_f32 v134, v134\n" \
  "v_exp_f32 v135, v135\n" \
  "v_exp_f32 v136, v136\n" \
  "v_exp_f32 v137, v137\n" \
  "v_exp_f32 v138, v138\n" \
  "v_exp_f32 v139, v139\n" \
  "v_exp_f32 v140, v140\n" \
  "v_exp_f32 v141, v141\n" \
  "s_waitcnt lgkmcnt(1)\n" \
  "v_smfmac_f32_32x32x32_bf16 v[192:207], v[232:235], v[128:135], v250\n" \
  "s_waitcnt vmcnt(5)\n" \
  "ds_write_b128 v27, v[96:99] offset:0\n" \
  "ds_write_b128 v27, v[100:103] offset:1024\n" \
  "v_exp_f32 v142, v142\n" \
  "v_exp_f32 v143, v143\n" \
  "v_exp_f32 v144, v144\n" \
  "v_exp_f32 v145, v145\n" \
  "v_smfmac_f32_32x32x32_bf16 v[192:207], v[236:239], v[136:143], v250\n" \
  "ds_write_b128 v27, v[104:107] offset:2048\n" \
  "ds_write_b128 v27, v[108:111] offset:3072\n" \
  "v_exp_f32 v146, v146\n" \
  "v_exp_f32 v147, v147\n" \
  "v_exp_f32 v148, v148\n" \
  "v_exp_f32 v149, v149\n" \
  "v_mfma_f32_32x32x16_bf16 v[160:175], v[224:227], v[240:243], v[160:175]\n" \
  "ds_read_b64_tr_b16 v[32:33], v26 offset:0\n" \
  "ds_read_b64_tr_b16 v[34:35], v26 offset:512\n" \
  "v_exp_f32 v150, v150\n" \
  "v_exp_f32 v151, v151\n" \
  "v_exp_f32 v152, v152\n" \
  "v_exp_f32 v153, v153\n" \
  "v_smfmac_f32_32x32x32_bf16 v[208:223], v[232:235], v[144:151], v250\n" \
  "ds_read_b128 v[232:235], v253 offset:36224\n" \
  "ds_read_b64_tr_b16 v[36:37], v26 offset:8\n" \
  "ds_read_b64_tr_b16 v[38:39], v26 offset:520\n" \
  "v_exp_f32 v154, v154\n" \
  "v_exp_f32 v155, v155\n" \
  "v_exp_f32 v156, v156\n" \
  "v_exp_f32 v157, v157\n" \
  "v_mfma_f32_32x32x16_bf16 v[176:191], v[224:227], v[244:247], v[176:191]\n" \
  "ds_read_b64 v[224:225], v251 offset:39936\n" \
  "ds_read_b64 v[226:227], v251 offset:40192\n" \
  "v_exp_f32 v158, v158\n" \
  "v_exp_f32 v159, v159\n" \
  "v_exp_f32 v160, v160\n" \
  "v_exp_f32 v161, v161\n" \
  "v_smfmac_f32_32x32x32_bf16 v[208:223], v[236:239], v[152:159], v250\n" \
  "ds_read_b128 v[236:239], v253 offset:36832\n" \
  "v_exp_f32 v162, v162\n" \
  "v_exp_f32 v163, v163\n" \
  "v_exp_f32 v164, v164\n" \
  "v_exp_f32 v165, v165\n" \
  "v_mfma_f32_32x32x4_2b_bf16 v[128:159], v[228:229], v[248:249], 0\n" \
  "ds_read_b32 v228, v252 offset:49920\n" \
  "v_exp_f32 v166, v166\n" \
  "v_exp_f32 v167, v167\n" \
  "v_exp_f32 v168, v168\n" \
  "v_exp_f32 v169, v169\n" \
  "v_exp_f32 v170, v170\n" \
  "v_exp_f32 v171, v171\n" \
  "v_exp_f32 v172, v172\n" \
  "v_exp_f32 v173, v173\n" \
  "s_waitcnt lgkmcnt(1)\n" \
  "v_smfmac_f32_32x32x32_bf16 v[192:207], v[232:235], v[160:167], v250\n" \
  "ds_read_b64_tr_b16 v[40:41], v26 offset:64\n" \
  "ds_read_b64_tr_b16 v[42:43], v26 offset:576\n" \
  "v_exp_f32 v174, v174\n" \
  "v_exp_f32 v175, v175\n" \
  "v_exp_f32 v176, v176\n" \
  "v_exp_f32 v177, v177\n" \
  "v_smfmac_f32_32x32x32_bf16 v[192:207], v[236:239], v[168:175], v250\n" \
  "ds_read_b64_tr_b16 v[44:45], v26 offset:72\n" \
  "ds_read_b64_tr_b16 v[46:47], v26 offset:584\n" \
  "v_exp_f32 v178, v178\n" \
  "v_exp_f32 v179, v179\n" \
  "v_exp_f32 v180, v180\n" \
  "v_exp_f32 v181, v181\n" \
  "v_mfma_f32_32x32x16_bf16 v[128:143], v[224:227], v[240:243], v[128:143]\n" \
  "s_add_u32 m0, s78, 0\n" \
  "s_nop 0\n" \
  "buffer_load_dwordx4 v16, s[60:63], s71 offen sc1 lds\n" \
  "v_exp_f32 v182, v182\n" \
  "v_exp_f32 v183, v183\n" \
  "v_exp_f32 v184, v184\n" \
  "v_exp_f32 v185, v185\n" \
  "v_smfmac_f32_32x32x32_bf16 v[208:223], v[232:235], v[176:183], v250\n" \
  "ds_read_b128 v[232:235], v253 offset:41216\n" \
  "s_add_u32 m0, s78, 8192\n" \
  "s_add_u32 s71, s71, 0x2000\n" \
  "buffer_load_dwordx4 v16, s[60:63], s71 offen sc1 lds\n" \
  "v_exp_f32 v186, v186\n" \
  "v_exp_f32 v187, v187\n" \
  "v_exp_f32 v188, v188\n" \
  "v_exp_f32 v189, v189\n" \
  "v_mfma_f32_32x32x16_bf16 v[144:159], v[224:227], v[244:247], v[144:159]\n" \
  "ds_read_b64 v[224:225], v251 offset:44928\n" \
  "ds_read_b64 v[226:227], v251 offset:45184\n" \
  "v_exp_f32 v190, v190\n" \
  "v_exp_f32 v191, v191\n" \
  "v_exp_f32 v128, v128\n" \
  "v_exp_f32 v129, v129\n" \
  "v_smfmac_f32_32x32x32_bf16 v[208:223], v[236:239], v[184:191], v250\n" \
  "ds_read_b128 v[236:239], v253 offset:41824\n" \
  "v_exp_f32 v130, v130\n" \
  "v_exp_f32 v131, v131\n" \
  "v_exp_f32 v132, v132\n" \
  "v_exp_f32 v133, v133\n" \
  "v_mfma_f32_32x32x4_2b_bf16 v[160:191], v[230:231], v[248:249], 0\n" \
  "ds_read_b32 v230, v252 offset:54912\n" \
  "v_exp_f32 v134, v134\n" \
  "v_exp_f32 v135, v135\n" \
  "v_exp_f32 v136, v136\n" \
  "v_exp_f32 v137, v137\n" \
  "v_exp_f32 v138, v138\n" \
  "v_exp_f32 v139, v139\n" \
  "v_exp_f32 v140, v140\n" \
  "v_exp_f32 v141, v141\n" \
  "s_waitcnt lgkmcnt(1)\n" \
  "v_smfmac_f32_32x32x32_bf16 v[192:207], v[232:235], v[128:135], v250\n" \
  "s_add_u32 m0, s78, 16384\n" \
  "s_add_u32 s71, s71, s73\n" \
  "buffer_load_dwordx4 v16, s[60:63], s71 offen sc1 lds\n" \
  "v_exp_f32 v142, v142\n" \
  "v_exp_f32 v143, v143\n" \
  "v_exp_f32 v144, v144\n" \
  "v_exp_f32 v145, v145\n" \
  "v_smfmac_f32_32x32x32_bf16 v[192:207], v[236:239], v[136:143], v250\n" \
  "s_add_u32 m0, s78, 24576\n" \
  "s_add_u32 s71, s71, s74\n" \
  "buffer_load_dwordx4 v16, s[60:63], s71 offen sc1 lds\n" \
  "v_exp_f32 v146, v146\n" \
  "v_exp_f32 v147, v147\n" \
  "v_exp_f32 v148, v148\n" \
  "v_exp_f32 v149, v149\n" \
  "v_mfma_f32_32x32x16_bf16 v[160:175], v[224:227], v[240:243], v[160:175]\n" \
  "s_add_u32 m0, s78, 32768\n" \
  "s_add_u32 s71, s71, 0x2000\n" \
  "buffer_load_dwordx4 v16, s[60:63], s71 offen sc1 lds\n" \
  "v_exp_f32 v150, v150\n" \
  "v_exp_f32 v151, v151\n" \
  "v_exp_f32 v152, v152\n" \
  "v_exp_f32 v153, v153\n" \
  "v_smfmac_f32_32x32x32_bf16 v[208:223], v[232:235], v[144:151], v250\n" \
  "ds_read_b128 v[232:235], v253 offset:46208\n" \
  "buffer_load_dwordx4 v[96:99], v16, s[60:63], s67 offen offset:0 sc1\n" \
  "v_exp_f32 v154, v154\n" \
  "v_exp_f32 v155, v155\n" \
  "v_exp_f32 v156, v156\n" \
  "v_exp_f32 v157, v157\n" \
  "v_mfma_f32_32x32x16_bf16 v[176:191], v[224:227], v[244:247], v[176:191]\n" \
  "ds_read_b64 v[224:225], v251 offset:49920\n" \
  "ds_read_b64 v[226:227], v251 offset:50176\n" \
  "v_exp_f32 v158, v158\n" \
  "v_exp_f32 v159, v159\n" \
  "v_exp_f32 v160, v160\n" \
  "v_exp_f32 v161, v161\n" \
  "v_smfmac_f32_32x32x32_bf16 v[208:223], v[236:239], v[152:159], v250\n" \
  "ds_read_b128 v[236:239], v253 offset:46816\n" \
  "v_exp_f32 v162, v162\n" \
  "v_exp_f32 v163, v163\n" \
  "v_exp_f32 v164, v164\n" \
  "v_exp_f32 v165, v165\n" \
  "v_mfma_f32_32x32x4_2b_bf16 v[128:159], v[228:229], v[248:249], 0\n" \
  "ds_read_b32 v228, v252 offset:59904\n" \
  "v_exp_f32 v166, v166\n" \
  "v_exp_f32 v167, v167\n" \
  "v_exp_f32 v168, v168\n" \
  "v_exp_f32 v169, v169\n" \
  "v_exp_f32 v170, v170\n" \
  "v_exp_f32 v171, v171\n" \
  "v_exp_f32 v172, v172\n" \
  "v_exp_f32 v173, v173\n" \
  "s_waitcnt lgkmcnt(1)\n" \
  "v_smfmac_f32_32x32x32_bf16 v[192:207], v[232:235], v[160:167], v250\n" \
  "s_waitcnt vmcnt(7)\n" \
  "ds_write_b128 v27, v[112:115] offset:0\n" \
  "ds_write_b128 v27, v[116:119] offset:1024\n" \
  "buffer_load_dwordx4 v[100:103], v16, s[60:63], s67 offen offset:1024 sc1\n" \
  "v_exp_f32 v174, v174\n" \
  "v_exp_f32 v175, v175\n" \
  "v_exp_f32 v176, v176\n" \
  "v_exp_f32 v177, v177\n" \
  "v_smfmac_f32_32x32x32_bf16 v[192:207], v[236:239], v[168:175], v250\n" \
  "ds_write_b128 v27, v[120:123] offset:2048\n" \
  "ds_write_b128 v27, v[124:127] offset:3072\n" \
  "buffer_load_dwordx4 v[104:107], v16, s[60:63], s67 offen offset:2048 sc1\n" \
  "v_exp_f32 v178, v178\n" \
  "v_exp_f32 v179, v179\n" \
  "v_exp_f32 v180, v180\n" \
  "v_exp_f32 v181, v181\n" \
  "v_mfma_f32_32x32x16_bf16 v[128:143], v[224:227], v[240:243], v[128:143]\n" \
  "ds_read_b64_tr_b16 v[112:113], v26 offset:0\n" \
  "ds_read_b64_tr_b16 v[114:115], v26 offset:512\n" \
  "buffer_load_dwordx4 v[108:111], v16, s[60:63], s67 offen offset:3072 sc1\n" \
  "v_exp_f32 v182, v182\n" \
  "v_exp_f32 v183, v183\n" \
  "v_exp_f32 v184, v184\n" \
  "v_exp_f32 v185, v185\n" \
  "v_smfmac_f32_32x32x32_bf16 v[208:223], v[232:235], v[176:183], v250\n" \
  "ds_read_b128 v[232:235], v253 offset:51200\n" \
  "ds_read_b64_tr_b16 v[116:117], v26 offset:8\n" \
  "ds_read_b64_tr_b16 v[118:119], v26 offset:520\n" \
  "v_exp_f32 v186, v186\n" \
  "v_exp_f32 v187, v187\n" \
  "v_exp_f32 v188, v188\n" \
  "v_exp_f32 v189, v189\n" \
  "v_mfma_f32_32x32x16_bf16 v[144:159], v[224:227], v[244:247], v[144:159]\n" \
  "ds_read_b64 v[224:225], v251 offset:54912\n" \
  "ds_read_b64 v[226:227], v251 offset:55168\n" \
  "v_add_u32 v251, 49920, v251\n" \
  "v_add_u32 v252, 49920, v252\n" \
  "v_add_u32 v253, 49920, v253\n" \
  "v_exp_f32 v190, v190\n" \
  "v_exp_f32 v191, v191\n" \
  "v_exp_f32 v128, v128\n" \
  "v_exp_f32 v129, v129\n" \
  "v_smfmac_f32_32x32x32_bf16 v[208:223], v[236:239], v[184:191], v250\n" \
  "ds_read_b128 v[236:239], v253 offset:1888\n" \
  "v_exp_f32 v130, v130\n" \
  "v_exp_f32 v131, v131\n" \
  "v_exp_f32 v132, v132\n" \
  "v_exp_f32 v133, v133\n" \
  "v_mfma_f32_32x32x4_2b_bf16 v[160:191], v[230:231], v[248:249], 0\n" \
  "ds_read_b32 v230, v252 offset:14976\n" \
  "v_exp_f32 v134, v134\n" \
  "v_exp_f32 v135, v135\n" \
  "v_exp_f32 v136, v136\n" \
  "v_exp_f32 v137, v137\n" \
  "v_exp_f32 v138, v138\n" \
  "v_exp_f32 v139, v139\n" \
  "v_exp_f32 v140, v140\n" \
  "v_exp_f32 v141, v141\n" \
  "s_waitcnt lgkmcnt(1)\n" \
  "v_smfmac_f32_32x32x32_bf16 v[192:207], v[232:235], v[128:135], v250\n" \
  "ds_read_b64_tr_b16 v[120:121], v26 offset:64\n" \
  "ds_read_b64_tr_b16 v[122:123], v26 offset:576\n" \
  "v_exp_f32 v142, v142\n" \
  "v_exp_f32 v143, v143\n" \
  "v_exp_f32 v144, v144\n" \
  "v_exp_f32 v145, v145\n" \
  "v_smfmac_f32_32x32x32_bf16 v[192:207], v[236:239], v[136:143], v250\n" \
  "ds_read_b64_tr_b16 v[124:125], v26 offset:72\n" \
  "ds_read_b64_tr_b16 v[126:127], v26 offset:584\n" \
  "v_exp_f32 v146, v146\n" \
  "v_exp_f32 v147, v147\n" \
  "v_exp_f32 v148, v148\n" \
  "v_exp_f32 v149, v149\n" \
  "v_mfma_f32_32x32x16_bf16 v[160:175], v[224:227], v[240:243], v[160:175]\n" \
  "v_exp_f32 v150, v150\n" \
  "v_exp_f32 v151, v151\n" \
  "v_exp_f32 v152, v152\n" \
  "v_exp_f32 v153, v153\n" \
  "v_smfmac_f32_32x32x32_bf16 v[208:223], v[232:235], v[144:151], v250\n" \
  "ds_read_b128 v[232:235], v253 offset:6272\n" \
  "v_exp_f32 v154, v154\n" \
  "v_exp_f32 v155, v155\n" \
  "v_exp_f32 v156, v156\n" \
  "v_exp_f32 v157, v157\n" \
  "v_mfma_f32_32x32x16_bf16 v[176:191], v[224:227], v[244:247], v[176:191]\n" \
  "ds_read_b64 v[224:225], v251 offset:9984\n" \
  "ds_read_b64 v[226:227], v251 offset:10240\n" \
  "v_exp_f32 v158, v158\n" \
  "v_exp_f32 v159, v159\n" \
  "v_exp_f32 v160, v160\n" \
  "v_exp_f32 v161, v161\n" \
  "v_smfmac_f32_32x32x32_bf16 v[208:223], v[236:239], v[152:159], v250\n" \
  "ds_read_b128 v[236:239], v253 offset:6880\n" \
  "v_exp_f32 v162, v162\n" \
  "v_exp_f32 v163, v163\n" \
  "v_exp_f32 v164, v164\n" \
  "v_exp_f32 v165, v165\n" \
  "v_mfma_f32_32x32x4_2b_bf16 v[128:159], v[228:229], v[248:249], 0\n" \
  "ds_read_b32 v228, v252 offset:19968\n" \
  "v_exp_f32 v166, v166\n" \
  "v_exp_f32 v167, v167\n" \
  "v_exp_f32 v168, v168\n" \
  "v_exp_f32 v169, v169\n" \
  "v_exp_f32 v170, v170\n" \
  "v_exp_f32 v171, v171\n" \
  "v_exp_f32 v172, v172\n" \
  "v_exp_f32 v173, v173\n" \
  "s_waitcnt lgkmcnt(1)\n" \
  "v_smfmac_f32_32x32x32_bf16 v[192:207], v[232:235], v[160:167], v250\n" \
  "buffer_store_dwordx4 v[32:35], v16, s[60:63], s88 offen sc1\n" \
  "v_exp_f32 v174, v174\n" \
  "v_exp_f32 v175, v175\n" \
  "v_exp_f32 v176, v176\n" \
  "v_exp_f32 v177, v177\n" \
  "v_smfmac_f32_32x32x32_bf16 v[192:207], v[236:239], v[168:175], v250\n" \
  "v_exp_f32 v178, v178\n" \
  "v_exp_f32 v179, v179\n" \
  "v_exp_f32 v180, v180\n" \
  "v_exp_f32 v181, v181\n" \
  "v_mfma_f32_32x32x16_bf16 v[128:143], v[224:227], v[240:243], v[128:143]\n" \
  "v_exp_f32 v182, v182\n" \
  "v_exp_f32 v183, v183\n" \
  "v_exp_f32 v184, v184\n" \
  "v_exp_f32 v185, v185\n" \
  "v_smfmac_f32_32x32x32_bf16 v[208:223], v[232:235], v[176:183], v250\n" \
  "ds_read_b128 v[232:235], v253 offset:11264\n" \
  "buffer_store_dwordx4 v[36:39], v16, s[60:63], s89 offen sc1\n" \
  "v_exp_f32 v186, v186\n" \
  "v_exp_f32 v187, v187\n" \
  "v_exp_f32 v188, v188\n" \
  "v_exp_f32 v189, v189\n" \
  "v_mfma_f32_32x32x16_bf16 v[144:159], v[224:227], v[244:247], v[144:159]\n" \
  "ds_read_b64 v[224:225], v251 offset:14976\n" \
  "ds_read_b64 v[226:227], v251 offset:15232\n" \
  "v_exp_f32 v190, v190\n" \
  "v_exp_f32 v191, v191\n" \
  "v_exp_f32 v128, v128\n" \
  "v_exp_f32 v129, v129\n" \
  "v_smfmac_f32_32x32x32_bf16 v[208:223], v[236:239], v[184:191], v250\n" \
  "ds_read_b128 v[236:239], v253 offset:11872\n" \
  "v_exp_f32 v130, v130\n" \
  "v_exp_f32 v131, v131\n" \
  "v_exp_f32 v132, v132\n" \
  "v_exp_f32 v133, v133\n" \
  "v_mfma_f32_32x32x4_2b_bf16 v[160:191], v[230:231], v[248:249], 0\n" \
  "ds_read_b32 v230, v252 offset:24960\n" \
  "v_exp_f32 v134, v134\n" \
  "v_exp_f32 v135, v135\n" \
  "v_exp_f32 v136, v136\n" \
  "v_exp_f32 v137, v137\n" \
  "v_exp_f32 v138, v138\n" \
  "v_exp_f32 v139, v139\n" \
  "v_exp_f32 v140, v140\n" \
  "v_exp_f32 v141, v141\n" \
  "s_waitcnt lgkmcnt(1)\n" \
  "v_smfmac_f32_32x32x32_bf16 v[192:207], v[232:235], v[128:135], v250\n" \
  "v_exp_f32 v142, v142\n" \
  "v_exp_f32 v143, v143\n" \
  "v_exp_f32 v144, v144\n" \
  "v_exp_f32 v145, v145\n" \
  "v_smfmac_f32_32x32x32_bf16 v[192:207], v[236:239], v[136:143], v250\n" \
  "v_exp_f32 v146, v146\n" \
  "v_exp_f32 v147, v147\n" \
  "v_exp_f32 v148, v148\n" \
  "v_exp_f32 v149, v149\n" \
  "v_mfma_f32_32x32x16_bf16 v[160:175], v[224:227], v[240:243], v[160:175]\n" \
  "buffer_store_dwordx4 v[40:43], v16, s[60:63], s90 offen sc1\n" \
  "v_exp_f32 v150, v150\n" \
  "v_exp_f32 v151, v151\n" \
  "v_exp_f32 v152, v152\n" \
  "v_exp_f32 v153, v153\n" \
  "v_smfmac_f32_32x32x32_bf16 v[208:223], v[232:235], v[144:151], v250\n" \
  "ds_read_b128 v[232:235], v253 offset:16256\n" \
  "v_exp_f32 v154, v154\n" \
  "v_exp_f32 v155, v155\n" \
  "v_exp_f32 v156, v156\n" \
  "v_exp_f32 v157, v157\n" \
  "v_mfma_f32_32x32x16_bf16 v[176:191], v[224:227], v[244:247], v[176:191]\n" \
  "ds_read_b64 v[224:225], v251 offset:19968\n" \
  "ds_read_b64 v[226:227], v251 offset:20224\n" \
  "v_exp_f32 v158, v158\n" \
  "v_exp_f32 v159, v159\n" \
  "v_exp_f32 v160, v160\n" \
  "v_exp_f32 v161, v161\n" \
  "v_smfmac_f32_32x32x32_bf16 v[208:223], v[236:239], v[152:159], v250\n" \
  "ds_read_b128 v[236:239], v253 offset:16864\n" \
  "v_exp_f32 v162, v162\n" \
  "v_exp_f32 v163, v163\n" \
  "v_exp_f32 v164, v164\n" \
  "v_exp_f32 v165, v165\n" \
  "v_mfma_f32_32x32x4_2b_bf16 v[128:159], v[228:229], v[248:249], 0\n" \
  "ds_read_b32 v228, v252 offset:29952\n" \
  "v_exp_f32 v166, v166\n" \
  "v_exp_f32 v167, v167\n" \
  "v_exp_f32 v168, v168\n" \
  "v_exp_f32 v169, v169\n" \
  "v_exp_f32 v170, v170\n" \
  "v_exp_f32 v171, v171\n" \
  "v_exp_f32 v172, v172\n" \
  "v_exp_f32 v173, v173\n" \
  "s_waitcnt lgkmcnt(1)\n" \
  "v_smfmac_f32_32x32x32_bf16 v[192:207], v[232:235], v[160:167], v250\n" \
  "v_exp_f32 v174, v174\n" \
  "v_exp_f32 v175, v175\n" \
  "v_exp_f32 v176, v176\n" \
  "v_exp_f32 v177, v177\n" \
  "v_smfmac_f32_32x32x32_bf16 v[192:207], v[236:239], v[168:175], v250\n" \
  "buffer_store_dwordx4 v[44:47], v16, s[60:63], s91 offen sc1\n" \
  "v_exp_f32 v178, v178\n" \
  "v_exp_f32 v179, v179\n" \
  "v_exp_f32 v180, v180\n" \
  "v_exp_f32 v181, v181\n" \
  "v_mfma_f32_32x32x16_bf16 v[128:143], v[224:227], v[240:243], v[128:143]\n" \
  "v_exp_f32 v182, v182\n" \
  "v_exp_f32 v183, v183\n" \
  "v_exp_f32 v184, v184\n" \
  "v_exp_f32 v185, v185\n" \
  "v_smfmac_f32_32x32x32_bf16 v[208:223], v[232:235], v[176:183], v250\n" \
  "ds_read_b128 v[232:235], v253 offset:21248\n" \
  "v_exp_f32 v186, v186\n" \
  "v_exp_f32 v187, v187\n" \
  "v_exp_f32 v188, v188\n" \
  "v_exp_f32 v189, v189\n" \
  "v_mfma_f32_32x32x16_bf16 v[144:159], v[224:227], v[244:247], v[144:159]\n" \
  "ds_read_b64 v[224:225], v251 offset:24960\n" \
  "ds_read_b64 v[226:227], v251 offset:25216\n" \
  "v_exp_f32 v190, v190\n" \
  "v_exp_f32 v191, v191\n" \
  "v_exp_f32 v128, v128\n" \
  "v_exp_f32 v129, v129\n" \
  "v_smfmac_f32_32x32x32_bf16 v[208:223], v[236:239], v[184:191], v250\n" \
  "ds_read_b128 v[236:239], v253 offset:21856\n" \
  "v_exp_f32 v130, v130\n" \
  "v_exp_f32 v131, v131\n" \
  "v_exp_f32 v132, v132\n" \
  "v_exp_f32 v133, v133\n" \
  "v_mfma_f32_32x32x4_2b_bf16 v[160:191], v[230:231], v[248:249], 0\n" \
  "ds_read_b32 v230, v252 offset:34944\n" \
  "v_exp_f32 v134, v134\n" \
  "v_exp_f32 v135, v135\n" \
  "v_exp_f32 v136, v136\n" \
  "v_exp_f32 v137, v137\n" \
  "v_exp_f32 v138, v138\n" \
  "v_exp_f32 v139, v139\n" \
  "v_exp_f32 v140, v140\n" \
  "v_exp_f32 v141, v141\n" \
  "s_waitcnt lgkmcnt(1)\n" \
  "v_smfmac_f32_32x32x32_bf16 v[192:207], v[232:235], v[128:135], v250\n" \
  "buffer_store_dwordx4 v[112:115], v16, s[60:63], s92 offen sc1\n" \
  "v_exp_f32 v142, v142\n" \
  "v_exp_f32 v143, v143\n" \
  "v_exp_f32 v144, v144\n" \
  "v_exp_f32 v145, v145\n" \
  "v_smfmac_f32_32x32x32_bf16 v[192:207], v[236:239], v[136:143], v250\n" \
  "v_exp_f32 v146, v146\n" \
  "v_exp_f32 v147, v147\n" \
  "v_exp_f32 v148, v148\n" \
  "v_exp_f32 v149, v149\n" \
  "v_mfma_f32_32x32x16_bf16 v[160:175], v[224:227], v[240:243], v[160:175]\n" \
  "v_exp_f32 v150, v150\n" \
  "v_exp_f32 v151, v151\n" \
  "v_exp_f32 v152, v152\n" \
  "v_exp_f32 v153, v153\n" \
  "v_smfmac_f32_32x32x32_bf16 v[208:223], v[232:235], v[144:151], v250\n" \
  "ds_read_b128 v[232:235], v253 offset:26240\n" \
  "buffer_store_dwordx4 v[116:119], v16, s[60:63], s93 offen sc1\n" \
  "v_exp_f32 v154, v154\n" \
  "v_exp_f32 v155, v155\n" \
  "v_exp_f32 v156, v156\n" \
  "v_exp_f32 v157, v157\n" \
  "v_mfma_f32_32x32x16_bf16 v[176:191], v[224:227], v[244:247], v[176:191]\n" \
  "ds_read_b64 v[224:225], v251 offset:29952\n" \
  "ds_read_b64 v[226:227], v251 offset:30208\n" \
  "v_exp_f32 v158, v158\n" \
  "v_exp_f32 v159, v159\n" \
  "v_exp_f32 v160, v160\n" \
  "v_exp_f32 v161, v161\n" \
  "v_smfmac_f32_32x32x32_bf16 v[208:223], v[236:239], v[152:159], v250\n" \
  "ds_read_b128 v[236:239], v253 offset:26848\n" \
  "v_exp_f32 v162, v162\n" \
  "v_exp_f32 v163, v163\n" \
  "v_exp_f32 v164, v164\n" \
  "v_exp_f32 v165, v165\n" \
  "v_mfma_f32_32x32x4_2b_bf16 v[128:159], v[228:229], v[248:249], 0\n" \
  "ds_read_b32 v228, v252 offset:39936\n" \
  "v_exp_f32 v166, v166\n" \
  "v_exp_f32 v167, v167\n" \
  "v_exp_f32 v168, v168\n" \
  "v_exp_f32 v169, v169\n" \
  "v_exp_f32 v170, v170\n" \
  "v_exp_f32 v171, v171\n" \
  "v_exp_f32 v172, v172\n" \
  "v_exp_f32 v173, v173\n" \
  "s_waitcnt lgkmcnt(1)\n" \
  "v_smfmac_f32_32x32x32_bf16 v[192:207], v[232:235], v[160:167], v250\n" \
  "v_exp_f32 v174, v174\n" \
  "v_exp_f32 v175, v175\n" \
  "v_exp_f32 v176, v176\n" \
  "v_exp_f32 v177, v177\n" \
  "v_smfmac_f32_32x32x32_bf16 v[192:207], v[236:239], v[168:175], v250\n" \
  "v_exp_f32 v178, v178\n" \
  "v_exp_f32 v179, v179\n" \
  "v_exp_f32 v180, v180\n" \
  "v_exp_f32 v181, v181\n" \
  "v_mfma_f32_32x32x16_bf16 v[128:143], v[224:227], v[240:243], v[128:143]\n" \
  "buffer_store_dwordx4 v[120:123], v16, s[60:63], s94 offen sc1\n" \
  "v_exp_f32 v182, v182\n" \
  "v_exp_f32 v183, v183\n" \
  "v_exp_f32 v184, v184\n" \
  "v_exp_f32 v185, v185\n" \
  "v_smfmac_f32_32x32x32_bf16 v[208:223], v[232:235], v[176:183], v250\n" \
  "ds_read_b128 v[232:235], v253 offset:31232\n" \
  "v_exp_f32 v186, v186\n" \
  "v_exp_f32 v187, v187\n" \
  "v_exp_f32 v188, v188\n" \
  "v_exp_f32 v189, v189\n" \
  "v_mfma_f32_32x32x16_bf16 v[144:159], v[224:227], v[244:247], v[144:159]\n" \
  "ds_read_b64 v[224:225], v251 offset:34944\n" \
  "ds_read_b64 v[226:227], v251 offset:35200\n" \
  "v_exp_f32 v190, v190\n" \
  "v_exp_f32 v191, v191\n" \
  "v_exp_f32 v128, v128\n" \
  "v_exp_f32 v129, v129\n" \
  "v_smfmac_f32_32x32x32_bf16 v[208:223], v[236:239], v[184:191], v250\n" \
  "ds_read_b128 v[236:239], v253 offset:31840\n" \
  "s_setprio 2\n" \
  "v_exp_f32 v130, v130\n" \
  "v_exp_f32 v131, v131\n" \
  "v_exp_f32 v132, v132\n" \
  "v_exp_f32 v133, v133\n" \
  "v_mfma_f32_32x32x4_2b_bf16 v[160:191], v[230:231], v[248:249], 0\n" \
  "ds_read_b32 v230, v252 offset:44928\n" \
  "v_exp_f32 v134, v134\n" \
  "v_exp_f32 v135, v135\n" \
  "v_exp_f32 v136, v136\n" \
  "v_exp_f32 v137, v137\n" \
  "v_exp_f32 v138, v138\n" \
  "v_exp_f32 v139, v139\n" \
  "v_exp_f32 v140, v140\n" \
  "v_exp_f32 v141, v141\n" \
  "s_waitcnt lgkmcnt(1)\n" \
  "v_smfmac_f32_32x32x32_bf16 v[192:207], v[232:235], v[128:135], v250\n" \
  "v_exp_f32 v142, v142\n" \
  "v_exp_f32 v143, v143\n" \
  "v_exp_f32 v144, v144\n" \
  "v_exp_f32 v145, v145\n" \
  "v_smfmac_f32_32x32x32_bf16 v[192:207], v[236:239], v[136:143], v250\n" \
  "buffer_store_dwordx4 v[124:127], v16, s[60:63], s95 offen sc1\n" \
  "v_exp_f32 v146, v146\n" \
  "v_exp_f32 v147, v147\n" \
  "v_exp_f32 v148, v148\n" \
  "v_exp_f32 v149, v149\n" \
  "v_mfma_f32_32x32x16_bf16 v[160:175], v[224:227], v[240:243], v[160:175]\n" \
  "v_exp_f32 v150, v150\n" \
  "v_exp_f32 v151, v151\n" \
  "v_exp_f32 v152, v152\n" \
  "v_exp_f32 v153, v153\n" \
  "v_smfmac_f32_32x32x32_bf16 v[208:223], v[232:235], v[144:151], v250\n" \
  "ds_read_b128 v[232:235], v253 offset:36224\n" \
  "v_exp_f32 v154, v154\n" \
  "v_exp_f32 v155, v155\n" \
  "v_exp_f32 v156, v156\n" \
  "v_exp_f32 v157, v157\n" \
  "v_mfma_f32_32x32x16_bf16 v[176:191], v[224:227], v[244:247], v[176:191]\n" \
  "ds_read_b64 v[224:225], v251 offset:39936\n" \
  "ds_read_b64 v[226:227], v251 offset:40192\n" \
  "v_exp_f32 v158, v158\n" \
  "v_exp_f32 v159, v159\n" \
  "v_exp_f32 v160, v160\n" \
  "v_exp_f32 v161, v161\n" \
  "v_smfmac_f32_32x32x32_bf16 v[208:223], v[236:239], v[152:159], v250\n" \
  "ds_read_b128 v[236:239], v253 offset:36832\n" \
  "v_exp_f32 v162, v162\n" \
  "v_exp_f32 v163, v163\n" \
  "v_exp_f32 v164, v164\n" \
  "v_exp_f32 v165, v165\n" \
  "v_mfma_f32_32x32x4_2b_bf16 v[128:159], v[228:229], v[248:249], 0\n" \
  "ds_read_b32 v228, v252 offset:49920\n" \
  "v_exp_f32 v166, v166\n" \
  "v_exp_f32 v167, v167\n" \
  "v_exp_f32 v168, v168\n" \
  "v_exp_f32 v169, v169\n" \
  "v_exp_f32 v170, v170\n" \
  "v_exp_f32 v171, v171\n" \
  "v_exp_f32 v172, v172\n" \
  "v_exp_f32 v173, v173\n" \
  "s_waitcnt lgkmcnt(1)\n" \
  "v_smfmac_f32_32x32x32_bf16 v[192:207], v[232:235], v[160:167], v250\n" \
  "v_mov_b64 v[32:33], -1\n" \
  "v_mov_b64 v[34:35], -1\n" \
  "buffer_store_dwordx4 v[32:35], v16, s[60:63], s65 offen offset:0 sc1\n" \
  "v_exp_f32 v174, v174\n" \
  "v_exp_f32 v175, v175\n" \
  "v_exp_f32 v176, v176\n" \
  "v_exp_f32 v177, v177\n" \
  "v_smfmac_f32_32x32x32_bf16 v[192:207], v[236:239], v[168:175], v250\n" \
  "v_exp_f32 v178, v178\n" \
  "v_exp_f32 v179, v179\n" \
  "v_exp_f32 v180, v180\n" \
  "v_exp_f32 v181, v181\n" \
  "v_mfma_f32_32x32x16_bf16 v[128:143], v[224:227], v[240:243], v[128:143]\n" \
  "v_exp_f32 v182, v182\n" \
  "v_exp_f32 v183, v183\n" \
  "v_exp_f32 v184, v184\n" \
  "v_exp_f32 v185, v185\n" \
  "v_smfmac_f32_32x32x32_bf16 v[208:223], v[232:235], v[176:183], v250\n" \
  "ds_read_b128 v[232:235], v253 offset:41216\n" \
  "buffer_load_dword v255, v31, s[60:63], 0 offen sc1\n" \
  "buffer_store_dwordx4 v[32:35], v16, s[60:63], s65 offen offset:1024 sc1\n" \
  "v_exp_f32 v186, v186\n" \
  "v_exp_f32 v187, v187\n" \
  "v_exp_f32 v188, v188\n" \
  "v_exp_f32 v189, v189\n" \
  "v_mfma_f32_32x32x16_bf16 v[144:159], v[224:227], v[244:247], v[144:159]\n" \
  "ds_read_b64 v[224:225], v251 offset:44928\n" \
  "ds_read_b64 v[226:227], v251 offset:45184\n" \
  "v_exp_f32 v190, v190\n" \
  "v_exp_f32 v191, v191\n" \
  "v_exp_f32 v128, v128\n" \
  "v_exp_f32 v129, v129\n" \
  "v_smfmac_f32_32x32x32_bf16 v[208:223], v[236:239], v[184:191], v250\n" \
  "ds_read_b128 v[236:239], v253 offset:41824\n" \
  "v_exp_f32 v130, v130\n" \
  "v_exp_f32 v131, v131\n" \
  "v_exp_f32 v132, v132\n" \
  "v_exp_f32 v133, v133\n" \
  "v_mfma_f32_32x32x4_2b_bf16 v[160:191], v[230:231], v[248:249], 0\n" \
  "ds_read_b32 v230, v252 offset:54912\n" \
  "v_exp_f32 v134, v134\n" \
  "v_exp_f32 v135, v135\n" \
  "v_exp_f32 v136, v136\n" \
  "v_exp_f32 v137, v137\n" \
  "v_exp_f32 v138, v138\n" \
  "v_exp_f32 v139, v139\n" \
  "v_exp_f32 v140, v140\n" \
  "v_exp_f32 v141, v141\n" \
  "s_waitcnt lgkmcnt(1)\n" \
  "v_smfmac_f32_32x32x32_bf16 v[192:207], v[232:235], v[128:135], v250\n" \
  "v_exp_f32 v142, v142\n" \
  "v_exp_f32 v143, v143\n" \
  "v_exp_f32 v144, v144\n" \
  "v_exp_f32 v145, v145\n" \
  "v_smfmac_f32_32x32x32_bf16 v[192:207], v[236:239], v[136:143], v250\n" \
  "v_exp_f32 v146, v146\n" \
  "v_exp_f32 v147, v147\n" \
  "v_exp_f32 v148, v148\n" \
  "v_exp_f32 v149, v149\n" \
  "v_mfma_f32_32x32x16_bf16 v[160:175], v[224:227], v[240:243], v[160:175]\n" \
  "buffer_store_dwordx4 v[32:35], v16, s[60:63], s65 offen offset:2048 sc1\n" \
  "v_exp_f32 v150, v150\n" \
  "v_exp_f32 v151, v151\n" \
  "v_exp_f32 v152, v152\n" \
  "v_exp_f32 v153, v153\n" \
  "v_smfmac_f32_32x32x32_bf16 v[208:223], v[232:235], v[144:151], v250\n" \
  "ds_read_b128 v[232:235], v253 offset:46208\n" \
  "v_exp_f32 v154, v154\n" \
  "v_exp_f32 v155, v155\n" \
  "v_exp_f32 v156, v156\n" \
  "v_exp_f32 v157, v157\n" \
  "v_mfma_f32_32x32x16_bf16 v[176:191], v[224:227], v[244:247], v[176:191]\n" \
  "ds_read_b64 v[224:225], v251 offset:49920\n" \
  "ds_read_b64 v[226:227], v251 offset:50176\n" \
  "v_exp_f32 v158, v158\n" \
  "v_exp_f32 v159, v159\n" \
  "v_exp_f32 v160, v160\n" \
  "v_exp_f32 v161, v161\n" \
  "v_smfmac_f32_32x32x32_bf16 v[208:223], v[236:239], v[152:159], v250\n" \
  "ds_read_b128 v[236:239], v253 offset:46816\n" \
  "s_setprio 1\n" \
  "v_exp_f32 v162, v162\n" \
  "v_exp_f32 v163, v163\n" \
  "v_exp_f32 v164, v164\n" \
  "v_exp_f32 v165, v165\n" \
  "v_mfma_f32_32x32x4_2b_bf16 v[128:159], v[228:229], v[248:249], 0\n" \
  "v_exp_f32 v166, v166\n" \
  "v_exp_f32 v167, v167\n" \
  "v_exp_f32 v168, v168\n" \
  "v_exp_f32 v169, v169\n" \
  "v_exp_f32 v170, v170\n" \
  "v_exp_f32 v171, v171\n" \
  "v_exp_f32 v172, v172\n" \
  "v_exp_f32 v173, v173\n" \
  "s_waitcnt lgkmcnt(0)\n" \
  "v_smfmac_f32_32x32x32_bf16 v[192:207], v[232:235], v[160:167], v250\n" \
  "v_exp_f32 v174, v174\n" \
  "v_exp_f32 v175, v175\n" \
  "v_exp_f32 v176, v176\n" \
  "v_exp_f32 v177, v177\n" \
  "v_smfmac_f32_32x32x32_bf16 v[192:207], v[236:239], v[168:175], v250\n" \
  "buffer_store_dwordx4 v[32:35], v16, s[60:63], s66 offen offset:0 sc1\n" \
  "v_exp_f32 v178, v178\n" \
  "v_exp_f32 v179, v179\n" \
  "v_exp_f32 v180, v180\n" \
  "v_exp_f32 v181, v181\n" \
  "v_mfma_f32_32x32x16_bf16 v[128:143], v[224:227], v[240:243], v[128:143]\n" \
  "v_exp_f32 v182, v182\n" \
  "v_exp_f32 v183, v183\n" \
  "v_exp_f32 v184, v184\n" \
  "v_exp_f32 v185, v185\n" \
  "v_smfmac_f32_32x32x32_bf16 v[208:223], v[232:235], v[176:183], v250\n" \
  "ds_read_b128 v[232:235], v253 offset:51200\n" \
  "v_exp_f32 v186, v186\n" \
  "v_exp_f32 v187, v187\n" \
  "v_exp_f32 v188, v188\n" \
  "v_exp_f32 v189, v189\n" \
  "v_mfma_f32_32x32x16_bf16 v[144:159], v[224:227], v[244:247], v[144:159]\n" \
  "ds_read_b64 v[224:225], v251 offset:54912\n" \
  "ds_read_b64 v[226:227], v251 offset:55168\n" \
  "v_add_u32 v251, 49920, v251\n" \
  "v_add_u32 v252, 49920, v252\n" \
  "v_add_u32 v253, 49920, v253\n" \
  "v_exp_f32 v190, v190\n" \
  "v_exp_f32 v191, v191\n" \
  "v_exp_f32 v128, v128\n" \
  "v_exp_f32 v129, v129\n" \
  "v_smfmac_f32_32x32x32_bf16 v[208:223], v[236:239], v[184:191], v250\n" \
  "ds_read_b128 v[236:239], v253 offset:1888\n" \
  "v_exp_f32 v130, v130\n" \
  "v_exp_f32 v131, v131\n" \
  "v_exp_f32 v132, v132\n" \
  "v_exp_f32 v133, v133\n" \
  "v_mfma_f32_32x32x4_2b_bf16 v[160:191], v[230:231], v[248:249], 0\n" \
  "v_exp_f32 v134, v134\n" \
  "v_exp_f32 v135, v135\n" \
  "v_exp_f32 v136, v136\n" \
  "v_exp_f32 v137, v137\n" \
  "v_exp_f32 v138, v138\n" \
  "v_exp_f32 v139, v139\n" \
  "v_exp_f32 v140, v140\n" \
  "v_exp_f32 v141, v141\n" \
  "s_waitcnt lgkmcnt(0)\n" \
  "v_smfmac_f32_32x32x32_bf16 v[192:207], v[232:235], v[128:135], v250\n" \
  "s_waitcnt vmcnt(3)\n" \
  "v_cmp_eq_u32 vcc, 0, v255\n" \
  "s_cbranch_vccnz 85f\n" \
  "84:\n" \
  "s_mov_b32 exec_hi, 0\n" \
  "buffer_store_dwordx4 v[32:35], v16, s[60:63], s66 offen offset:1024 sc1\n" \
  "s_mov_b64 exec, -1\n" \
  "v_exp_f32 v142, v142\n" \
  "v_exp_f32 v143, v143\n" \
  "v_exp_f32 v144, v144\n" \
  "v_exp_f32 v145, v145\n" \
  "v_smfmac_f32_32x32x32_bf16 v[192:207], v[236:239], v[136:143], v250\n" \
  "v_exp_f32 v146, v146\n" \
  "v_exp_f32 v147, v147\n" \
  "v_exp_f32 v148, v148\n" \
  "v_exp_f32 v149, v149\n" \
  "v_mfma_f32_32x32x16_bf16 v[160:175], v[224:227], v[240:243], v[160:175]\n" \
  "v_exp_f32 v150, v150\n" \
  "v_exp_f32 v151, v151\n" \
  "v_exp_f32 v152, v152\n" \
  "v_exp_f32 v153, v153\n" \
  "v_smfmac_f32_32x32x32_bf16 v[208:223], v[232:235], v[144:151], v250\n" \
  "ds_read_b128 v[232:235], v253 offset:6272\n" \
  "v_exp_f32 v154, v154\n" \
  "v_exp_f32 v155, v155\n" \
  "v_exp_f32 v156, v156\n" \
  "v_exp_f32 v157, v157\n" \
  "v_mfma_f32_32x32x16_bf16 v[176:191], v[224:227], v[244:247], v[176:191]\n" \
  "v_exp_f32 v158, v158\n" \
  "v_exp_f32 v159, v159\n" \
  "v_exp_f32 v160, v160\n" \
  "v_exp_f32 v161, v161\n" \
  "v_smfmac_f32_32x32x32_bf16 v[208:223], v[236:239], v[152:159], v250\n" \
  "ds_read_b128 v[236:239], v253 offset:6880\n" \
  "s_setprio 0\n" \
  "v_exp_f32 v162, v162\n" \
  "v_exp_f32 v163, v163\n" \
  "v_exp_f32 v164, v164\n" \
  "v_exp_f32 v165, v165\n" \
  "v_exp_f32 v166, v166\n" \
  "v_exp_f32 v167, v167\n" \
  "v_exp_f32 v168, v168\n" \
  "v_exp_f32 v169, v169\n" \
  "v_exp_f32 v170, v170\n" \
  "v_exp_f32 v171, v171\n" \
  "v_exp_f32 v172, v172\n" \
  "v_exp_f32 v173, v173\n" \
  "s_waitcnt lgkmcnt(0)\n" \
  "v_smfmac_f32_32x32x32_bf16 v[192:207], v[232:235], v[160:167], v250\n" \
  "v_exp_f32 v174, v174\n" \
  "v_exp_f32 v175, v175\n" \
  "v_exp_f32 v176, v176\n" \
  "v_exp_f32 v177, v177\n" \
  "v_smfmac_f32_32x32x32_bf16 v[192:207], v[236:239], v[168:175], v250\n" \
  "v_exp_f32 v178, v178\n" \
  "v_exp_f32 v179, v179\n" \
  "v_exp_f32 v180, v180\n" \
  "v_exp_f32 v181, v181\n" \
  "v_exp_f32 v182, v182\n" \
  "v_exp_f32 v183, v183\n" \
  "v_exp_f32 v184, v184\n" \
  "v_exp_f32 v185, v185\n" \
  "v_smfmac_f32_32x32x32_bf16 v[208:223], v[232:235], v[176:183], v250\n" \
  "v_exp_f32 v186, v186\n" \
  "v_exp_f32 v187, v187\n" \
  "v_exp_f32 v188, v188\n" \
  "v_exp_f32 v189, v189\n" \
  "v_exp_f32 v190, v190\n" \
  "v_exp_f32 v191, v191\n" \
  "s_nop 1\n" \
  "v_smfmac_f32_32x32x32_bf16 v[208:223], v[236:239], v[184:191], v250\n" \
  "s_nop 11\n" \
  "ds_write_b128 v27, v[208:211]\n" \
  "ds_write_b128 v27, v[212:215] offset:1024\n" \
  "ds_write_b128 v27, v[216:219] offset:2048\n" \
  "s_waitcnt vmcnt(0) lgkmcnt(0)\n" \
  "s_barrier\n" \
  "buffer_store_dword v250, v17, s[60:63], 0 offen sc1\n" \
  "ds_read_b128 v[208:211], v23\n" \
  "ds_read_b128 v[212:215], v23 offset:1024\n" \
  "ds_read_b128 v[216:219], v23 offset:2048\n" \
  "s_waitcnt lgkmcnt(0)\n" \
  "v_add_f32 v192, v192, v208\n" \
  "v_add_f32 v193, v193, v209\n" \
  "v_add_f32 v194, v194, v210\n" \
  "v_add_f32 v195, v195, v211\n" \
  "v_add_f32 v196, v196, v212\n" \
  "v_add_f32 v197, v197, v213\n" \
  "v_add_f32 v198, v198, v214\n" \
  "v_add_f32 v199, v199, v215\n" \
  "v_add_f32 v200, v200, v216\n" \
  "v_add_f32 v201, v201, v217\n" \
  "v_add_f32 v202, v202, v218\n" \
  "s_branch 87f\n" \
  "85:\n" \
  "s_mov_b32 s75, 0x40000\n" \
  "86:\n" \
  "s_sleep 1\n" \
  "buffer_load_dword v255, v31, s[60:63], 0 offen sc1\n" \
  "s_sub_u32 s75, s75, 1\n" \
  "s_waitcnt vmcnt(0)\n" \
  "s_cbranch_scc1 84b\n" \
  "v_cmp_eq_u32 vcc, 0, v255\n" \
  "s_cbranch_vccnz 86b\n" \
  "s_branch 84b\n" \
  "87:\n"
#define ATTN_CLOB "v32","v33","v34","v35","v36","v37","v38","v39","v40","v41","v42","v43","v44","v45","v46","v47","v112","v113","v114","v115","v116","v117","v118","v119","v120","v121","v122","v123","v124","v125","v126","v127","v128","v129","v130","v131","v132","v133","v134","v135","v136","v137","v138","v139","v140","v141","v142","v143","v144","v145","v146","v147","v148","v149","v150","v151","v152","v153","v154","v155","v156","v157","v158","v159","v160","v161","v162","v163","v164","v165","v166","v167","v168","v169","v170","v171","v172","v173","v174","v175","v176","v177","v178","v179","v180","v181","v182","v183","v184","v185","v186","v187","v188","v189","v190","v191","v208","v209","v210","v211","v212","v213","v214","v215","v216","v217","v218","v219","v220","v221","v222","v223","v224","v225","v226","v227","v228","v229","v230","v231","v232","v233","v234","v235","v236","v237","v238","v239","v240","v241","v242","v243","v244","v245","v246","v247","v248","v249","v250","v251","v252","v253","v254","v255","s80","s81","s82","s83","s84","s85","s86","s87","s88","s89","s90","s91","s92","s93","s94","s95"
#define FFN_PF 7
#define FFN_ASM \
  "s_nop 4\n" \
  "v_mov_b32 v7, 1\n" \
  "s_mov_b32 m0, s69\n" \
  "s_waitcnt vmcnt(0) lgkmcnt(0)\n" \
  "v_cmp_eq_u32 vcc, 0, v6\n" \
  "s_cbranch_vccnz 48f\n" \
  "38:\n" \
  "s_barrier\n" \
  "buffer_store_dword v7, v17, s[64:67], 0 offen sc1\n" \
  "buffer_load_dwordx4 v8, s[64:67], s68 offen sc1 lds\n" \
  "s_add_u32 m0, m0, 0x2000\n" \
  "s_add_u32 s68, s68, 0x2000\n" \
  "buffer_load_dwordx4 v8, s[64:67], s68 offen sc1 lds\n" \
  "s_add_u32 m0, m0, 0x2000\n" \
  "s_add_u32 s68, s68, 0x2000\n" \
  "buffer_load_dwordx4 v8, s[64:67], s68 offen sc1 lds\n" \
  "s_add_u32 m0, m0, 0x2000\n" \
  "s_add_u32 s68, s68, 0x2000\n" \
  "buffer_load_dwordx4 v8, s[64:67], s68 offen sc1 lds\n" \
  "s_add_u32 m0, m0, 0x2000\n" \
  "s_add_u32 s68, s68, 0x2000\n" \
  "buffer_load_dwordx4 v8, s[64:67], s68 offen sc1 lds\n" \
  "s_add_u32 m0, m0, 0x2000\n" \
  "s_add_u32 s68, s68, 0x2000\n" \
  "buffer_load_dwordx4 v8, s[64:67], s68 offen sc1 lds\n" \
  "s_add_u32 m0, m0, 0x2000\n" \
  "s_add_u32 s68, s68, 0x2000\n" \
  "buffer_load_dwordx4 v8, s[64:67], s68 offen sc1 lds\n" \
  "buffer_load_dword v6, v18, s[64:67], 0 offen sc1\n" \
  "s_waitcnt vmcnt(0)\n" \
  "v_cmp_eq_u32 vcc, 0, v6\n" \
  "s_cbranch_vccnz 49f\n" \
  "39:\n" \
  "buffer_load_dwordx4 v[112:115], v8, s[64:67], s72 offen offset:0 sc1\n" \
  "buffer_load_dwordx4 v[116:119], v8, s[64:67], s72 offen offset:1024 sc1\n" \
  "buffer_load_dwordx4 v[120:123], v8, s[64:67], s72 offen offset:2048 sc1\n" \
  "buffer_load_dwordx4 v[124:127], v8, s[64:67], s72 offen offset:3072 sc1\n" \
  "v_add_u32 v4, 49152, v8\n" \
  "v_add_u32 v5, 98304, v8\n" \
  "v_add_u32 v3, s79, v8\n" \
  "s_barrier\n" \
  "ds_read_b128 v[16:19], v8 offset:0\n" \
  "ds_read_b128 v[20:23], v8 offset:1024\n" \
  "ds_read_b128 v[24:27], v8 offset:2048\n" \
  "ds_read_b128 v[28:31], v8 offset:3072\n" \
  "s_add_u32 s72, s72, 0x1000\n" \
  "buffer_load_dwordx4 v[128:131], v8, s[64:67], s72 offen offset:0 sc1\n" \
  "buffer_load_dwordx4 v[132:135], v8, s[64:67], s72 offen offset:1024 sc1\n" \
  "buffer_load_dwordx4 v[136:139], v8, s[64:67], s72 offen offset:2048 sc1\n" \
  "buffer_load_dwordx4 v[140:143], v8, s[64:67], s72 offen offset:3072 sc1\n" \
  "s_waitcnt vmcnt(6)\n" \
  "v_swap_b32 v114, v116\n" \
  "v_swap_b32 v115, v117\n" \
  "s_nop 0\n" \
  "s_waitcnt lgkmcnt(3)\n" \
  "v_mfma_f32_16x16x32_bf16 v[44:47], v[16:19], v[112:115], 0\n" \
  "ds_read_b128 v[32:35], v8 offset:4096\n" \
  "s_setprio 3\n" \
  "v_mfma_f32_16x16x32_bf16 v[48:51], v[16:19], v[116:119], 0\n" \
  "s_waitcnt lgkmcnt(3)\n" \
  "v_mfma_f32_16x16x32_bf16 v[60:63], v[20:23], v[112:115], 0\n" \
  "ds_read_b128 v[16:19], v8 offset:5120\n" \
  "v_mfma_f32_16x16x32_bf16 v[64:67], v[20:23], v[116:119], 0\n" \
  "s_waitcnt lgkmcnt(3)\n" \
  "v_mfma_f32_16x16x32_bf16 v[52:55], v[24:27], v[112:115], 0\n" \
  "ds_read_b128 v[20:23], v8 offset:6144\n" \
  "v_mfma_f32_16x16x32_bf16 v[56:59], v[24:27], v[116:119], 0\n" \
  "s_add_u32 s72, s72, 0x1000\n" \
  "buffer_load_dwordx4 v[144:147], v8, s[64:67], s72 offen offset:0 sc1\n" \
  "s_waitcnt lgkmcnt(3)\n" \
  "v_mfma_f32_16x16x32_bf16 v[68:71], v[28:31], v[112:115], 0\n" \
  "ds_read_b128 v[24:27], v8 offset:7168\n" \
  "v_mfma_f32_16x16x32_bf16 v[72:75], v[28:31], v[116:119], 0\n" \
  "s_waitcnt lgkmcnt(3)\n" \
  "v_mfma_f32_16x16x32_bf16 v[76:79], v[32:35], v[112:115], 0\n" \
  "ds_read_b128 v[28:31], v8 offset:8192\n" \
  "v_mfma_f32_16x16x32_bf16 v[80:83], v[32:35], v[116:119], 0\n" \
  "buffer_load_dwordx4 v[148:151], v8, s[64:67], s72 offen offset:1024 sc1\n" \
  "s_waitcnt vmcnt(6)\n" \
  "v_swap_b32 v122, v124\n" \
  "v_swap_b32 v123, v125\n" \
  "s_waitcnt lgkmcnt(3)\n" \
  "v_mfma_f32_16x16x32_bf16 v[84:87], v[16:19], v[112:115], 0\n" \
  "ds_read_b128 v[32:35], v8 offset:9216\n" \
  "v_mfma_f32_16x16x32_bf16 v[88:91], v[16:19], v[116:119], 0\n" \
  "s_waitcnt lgkmcnt(3)\n" \
  "v_mfma_f32_16x16x32_bf16 v[44:47], v[20:23], v[120:123], v[44:47]\n" \
  "ds_read_b128 v[16:19], v8 offset:10240\n" \
  "v_mfma_f32_16x16x32_bf16 v[48:51], v[20:23], v[124:127], v[48:51]\n" \
  "buffer_load_dwordx4 v[152:155], v8, s[64:67], s72 offen offset:2048 sc1\n" \
  "s_waitcnt lgkmcnt(3)\n" \
  "v_mfma_f32_16x16x32_bf16 v[60:63], v[24:27], v[120:123], v[60:63]\n" \
  "ds_read_b128 v[20:23], v8 offset:11264\n" \
  "v_mfma_f32_16x16x32_bf16 v[64:67], v[24:27], v[124:127], v[64:67]\n" \
  "s_waitcnt lgkmcnt(3)\n" \
  "v_mfma_f32_16x16x32_bf16 v[52:55], v[28:31], v[120:123], v[52:55]\n" \
  "ds_read_b128 v[24:27], v8 offset:12288\n" \
  "v_mfma_f32_16x16x32_bf16 v[56:59], v[28:31], v[124:127], v[56:59]\n" \
  "s_waitcnt lgkmcnt(3)\n" \
  "v_mfma_f32_16x16x32_bf16 v[68:71], v[32:35], v[120:123], v[68:71]\n" \
  "ds_read_b128 v[28:31], v8 offset:18432\n" \
  "v_mfma_f32_16x16x32_bf16 v[72:75], v[32:35], v[124:127], v[72:75]\n" \
  "buffer_load_dwordx4 v[156:159], v8, s[64:67], s72 offen offset:3072 sc1\n" \
  "s_waitcnt lgkmcnt(3)\n" \
  "v_mfma_f32_16x16x32_bf16 v[76:79], v[16:19], v[120:123], v[76:79]\n" \
  "ds_read_b128 v[32:35], v8 offset:13312\n" \
  "v_mfma_f32_16x16x32_bf16 v[80:83], v[16:19], v[124:127], v[80:83]\n" \
  "s_waitcnt vmcnt(6)\n" \
  "v_swap_b32 v130, v132\n" \
  "v_swap_b32 v131, v133\n" \
  "s_waitcnt vmcnt(4)\n" \
  "v_swap_b32 v138, v140\n" \
  "v_swap_b32 v139, v141\n" \
  "s_waitcnt lgkmcnt(3)\n" \
  "v_mfma_f32_16x16x32_bf16 v[84:87], v[20:23], v[120:123], v[84:87]\n" \
  "ds_read_b128 v[16:19], v8 offset:19456\n" \
  "v_mfma_f32_16x16x32_bf16 v[88:91], v[20:23], v[124:127], v[88:91]\n" \
  "s_add_u32 s72, s72, 0x1000\n" \
  "buffer_load_dwordx4 v[160:163], v8, s[64:67], s72 offen offset:0 sc1\n" \
  "s_waitcnt lgkmcnt(2)\n" \
  "v_mfma_f32_16x16x32_bf16 v[44:47], v[24:27], v[128:131], v[44:47]\n" \
  "ds_read_b128 v[20:23], v8 offset:14336\n" \
  "v_mfma_f32_16x16x32_bf16 v[44:47], v[28:31], v[136:139], v[44:47]\n" \
  "v_mfma_f32_16x16x32_bf16 v[48:51], v[24:27], v[132:135], v[48:51]\n" \
  "ds_read_b128 v[24:27], v8 offset:20480\n" \
  "v_mfma_f32_16x16x32_bf16 v[48:51], v[28:31], v[140:143], v[48:51]\n" \
  "buffer_load_dwordx4 v[164:167], v8, s[64:67], s72 offen offset:1024 sc1\n" \
  "s_waitcnt lgkmcnt(2)\n" \
  "v_mfma_f32_16x16x32_bf16 v[60:63], v[32:35], v[128:131], v[60:63]\n" \
  "ds_read_b128 v[28:31], v8 offset:15360\n" \
  "v_mfma_f32_16x16x32_bf16 v[60:63], v[16:19], v[136:139], v[60:63]\n" \
  "v_mfma_f32_16x16x32_bf16 v[64:67], v[32:35], v[132:135], v[64:67]\n" \
  "ds_read_b128 v[32:35], v8 offset:21504\n" \
  "v_mfma_f32_16x16x32_bf16 v[64:67], v[16:19], v[140:143], v[64:67]\n" \
  "s_waitcnt lgkmcnt(2)\n" \
  "v_mfma_f32_16x16x32_bf16 v[52:55], v[20:23], v[128:131], v[52:55]\n" \
  "ds_read_b128 v[16:19], v8 offset:16384\n" \
  "v_mfma_f32_16x16x32_bf16 v[52:55], v[24:27], v[136:139], v[52:55]\n" \
  "buffer_load_dwordx4 v[168:171], v8, s[64:67], s72 offen offset:2048 sc1\n" \
  "v_mfma_f32_16x16x32_bf16 v[56:59], v[20:23], v[132:135], v[56:59]\n" \
  "ds_read_b128 v[20:23], v8 offset:22528\n" \
  "v_mfma_f32_16x16x32_bf16 v[56:59], v[24:27], v[140:143], v[56:59]\n" \
  "s_waitcnt lgkmcnt(2)\n" \
  "v_mfma_f32_16x16x32_bf16 v[68:71], v[28:31], v[128:131], v[68:71]\n" \
  "ds_read_b128 v[24:27], v8 offset:17408\n" \
  "v_mfma_f32_16x16x32_bf16 v[68:71], v[32:35], v[136:139], v[68:71]\n" \
  "buffer_load_dwordx4 v[172:175], v8, s[64:67], s72 offen offset:3072 sc1\n" \
  "v_mfma_f32_16x16x32_bf16 v[72:75], v[28:31], v[132:135], v[72:75]\n" \
  "ds_read_b128 v[28:31], v8 offset:23552\n" \
  "s_setprio 2\n" \
  "v_mfma_f32_16x16x32_bf16 v[72:75], v[32:35], v[140:143], v[72:75]\n" \
  "s_waitcnt lgkmcnt(2)\n" \
  "v_mfma_f32_16x16x32_bf16 v[76:79], v[16:19], v[128:131], v[76:79]\n" \
  "ds_read_b128 v[32:35], v8 offset:24576\n" \
  "v_mfma_f32_16x16x32_bf16 v[76:79], v[20:23], v[136:139], v[76:79]\n" \
  "s_add_u32 m0, s69, 57344\n" \
  "s_add_u32 s68, s68, 0x2000\n" \
  "buffer_load_dwordx4 v8, s[64:67], s68 offen sc1 lds\n" \
  "v_mfma_f32_16x16x32_bf16 v[80:83], v[16:19], v[132:135], v[80:83]\n" \
  "ds_read_b128 v[16:19], v8 offset:30720\n" \
  "v_mfma_f32_16x16x32_bf16 v[80:83], v[20:23], v[140:143], v[80:83]\n" \
  "s_waitcnt lgkmcnt(2)\n" \
  "v_mfma_f32_16x16x32_bf16 v[84:87], v[24:27], v[128:131], v[84:87]\n" \
  "ds_read_b128 v[20:23], v8 offset:25600\n" \
  "v_mfma_f32_16x16x32_bf16 v[84:87], v[28:31], v[136:139], v[84:87]\n" \
  "s_add_u32 m0, s69, 65536\n" \
  "s_add_u32 s68, s68, 0x2000\n" \
  "buffer_load_dwordx4 v8, s[64:67], s68 offen sc1 lds\n" \
  "s_waitcnt vmcnt(8)\n" \
  "v_swap_b32 v146, v148\n" \
  "v_swap_b32 v147, v149\n" \
  "s_waitcnt vmcnt(6)\n" \
  "v_swap_b32 v154, v156\n" \
  "v_swap_b32 v155, v157\n" \
  "v_mfma_f32_16x16x32_bf16 v[88:91], v[24:27], v[132:135], v[88:91]\n" \
  "ds_read_b128 v[24:27], v8 offset:31744\n" \
  "v_mfma_f32_16x16x32_bf16 v[88:91], v[28:31], v[140:143], v[88:91]\n" \
  "s_waitcnt lgkmcnt(2)\n" \
  "v_mfma_f32_16x16x32_bf16 v[44:47], v[32:35], v[144:147], v[44:47]\n" \
  "ds_read_b128 v[28:31], v8 offset:26624\n" \
  "v_mfma_f32_16x16x32_bf16 v[44:47], v[16:19], v[152:155], v[44:47]\n" \
  "v_mfma_f32_16x16x32_bf16 v[48:51], v[32:35], v[148:151], v[48:51]\n" \
  "ds_read_b128 v[32:35], v8 offset:32768\n" \
  "v_mfma_f32_16x16x32_bf16 v[48:51], v[16:19], v[156:159], v[48:51]\n" \
  "s_add_u32 m0, s69, 73728\n" \
  "s_add_u32 s68, s68, 0x2000\n" \
  "buffer_load_dwordx4 v8, s[64:67], s68 offen sc1 lds\n" \
  "s_waitcnt lgkmcnt(2)\n" \
  "v_mfma_f32_16x16x32_bf16 v[60:63], v[20:23], v[144:147], v[60:63]\n" \
  "ds_read_b128 v[16:19], v8 offset:27648\n" \
  "v_mfma_f32_16x16x32_bf16 v[60:63], v[24:27], v[152:155], v[60:63]\n" \
  "v_mfma_f32_16x16x32_bf16 v[64:67], v[20:23], v[148:151], v[64:67]\n" \
  "ds_read_b128 v[20:23], v8 offset:33792\n" \
  "v_mfma_f32_16x16x32_bf16 v[64:67], v[24:27], v[156:159], v[64:67]\n" \
  "s_add_u32 m0, s69, 81920\n" \
  "s_add_u32 s68, s68, 0x2000\n" \
  "buffer_load_dwordx4 v8, s[64:67], s68 offen sc1 lds\n" \
  "s_waitcnt lgkmcnt(2)\n" \
  "v_mfma_f32_16x16x32_bf16 v[52:55], v[28:31], v[144:147], v[52:55]\n" \
  "ds_read_b128 v[24:27], v8 offset:28672\n" \
  "v_mfma_f32_16x16x32_bf16 v[52:55], v[32:35], v[152:155], v[52:55]\n" \
  "v_mfma_f32_16x16x32_bf16 v[56:59], v[28:31], v[148:151], v[56:59]\n" \
  "ds_read_b128 v[28:31], v8 offset:34816\n" \
  "v_mfma_f32_16x16x32_bf16 v[56:59], v[32:35], v[156:159], v[56:59]\n" \
  "s_add_u32 s72, s72, 0x1000\n" \
  "buffer_load_dwordx4 v[176:179], v8, s[64:67], s72 offen offset:0 sc1\n" \
  "s_waitcnt lgkmcnt(2)\n" \
  "v_mfma_f32_16x16x32_bf16 v[68:71], v[16:19], v[144:147], v[68:71]\n" \
  "ds_read_b128 v[32:35], v8 offset:29696\n" \
  "v_mfma_f32_16x16x32_bf16 v[68:71], v[20:23], v[152:155], v[68:71]\n" \
  "v_mfma_f32_16x16x32_bf16 v[72:75], v[16:19], v[148:151], v[72:75]\n" \
  "ds_read_b128 v[16:19], v8 offset:35840\n" \
  "v_mfma_f32_16x16x32_bf16 v[72:75], v[20:23], v[156:159], v[72:75]\n" \
  "s_waitcnt lgkmcnt(2)\n" \
  "v_mfma_f32_16x16x32_bf16 v[76:79], v[24:27], v[144:147], v[76:79]\n" \
  "ds_read_b128 v[20:23], v8 offset:36864\n" \
  "v_mfma_f32_16x16x32_bf16 v[76:79], v[28:31], v[152:155], v[76:79]\n" \
  "buffer_load_dwordx4 v[180:183], v8, s[64:67], s72 offen offset:1024 sc1\n" \
  "v_mfma_f32_16x16x32_bf16 v[80:83], v[24:27], v[148:151], v[80:83]\n" \
  "ds_read_b128 v[24:27], v8 offset:43008\n" \
  "s_setprio 1\n" \
  "v_mfma_f32_16x16x32_bf16 v[80:83], v[28:31], v[156:159], v[80:83]\n" \
  "s_waitcnt lgkmcnt(2)\n" \
  "v_mfma_f32_16x16x32_bf16 v[84:87], v[32:35], v[144:147], v[84:87]\n" \
  "ds_read_b128 v[28:31], v8 offset:37888\n" \
  "v_mfma_f32_16x16x32_bf16 v[84:87], v[16:19], v[152:155], v[84:87]\n" \
  "buffer_load_dwordx4 v[184:187], v8, s[64:67], s72 offen offset:2048 sc1\n" \
  "s_waitcnt vmcnt(9)\n" \
  "v_swap_b32 v162, v164\n" \
  "v_swap_b32 v163, v165\n" \
  "s_waitcnt vmcnt(7)\n" \
  "v_swap_b32 v170, v172\n" \
  "v_swap_b32 v171, v173\n" \
  "v_mfma_f32_16x16x32_bf16 v[88:91], v[32:35], v[148:151], v[88:91]\n" \
  "ds_read_b128 v[32:35], v8 offset:44032\n" \
  "v_mfma_f32_16x16x32_bf16 v[88:91], v[16:19], v[156:159], v[88:91]\n" \
  "s_waitcnt lgkmcnt(2)\n" \
  "v_mfma_f32_16x16x32_bf16 v[44:47], v[20:23], v[160:163], v[44:47]\n" \
  "ds_read_b128 v[16:19], v8 offset:38912\n" \
  "v_mfma_f32_16x16x32_bf16 v[44:47], v[24:27], v[168:171], v[44:47]\n" \
  "buffer_load_dwordx4 v[188:191], v8, s[64:67], s72 offen offset:3072 sc1\n" \
  "v_mfma_f32_16x16x32_bf16 v[48:51], v[20:23], v[164:167], v[48:51]\n" \
  "ds_read_b128 v[20:23], v8 offset:45056\n" \
  "v_mfma_f32_16x16x32_bf16 v[48:51], v[24:27], v[172:175], v[48:51]\n" \
  "s_waitcnt lgkmcnt(2)\n" \
  "v_mfma_f32_16x16x32_bf16 v[60:63], v[28:31], v[160:163], v[60:63]\n" \
  "ds_read_b128 v[24:27], v8 offset:39936\n" \
  "v_mfma_f32_16x16x32_bf16 v[60:63], v[32:35], v[168:171], v[60:63]\n" \
  "v_mfma_f32_16x16x32_bf16 v[64:67], v[28:31], v[164:167], v[64:67]\n" \
  "ds_read_b128 v[28:31], v8 offset:46080\n" \
  "s_setprio 0\n" \
  "v_mfma_f32_16x16x32_bf16 v[64:67], v[32:35], v[172:175], v[64:67]\n" \
  "s_add_u32 s72, s72, 0x1000\n" \
  "buffer_load_dwordx4 v[192:195], v8, s[64:67], s72 offen offset:0 sc1\n" \
  "s_waitcnt lgkmcnt(2)\n" \
  "v_mfma_f32_16x16x32_bf16 v[52:55], v[16:19], v[160:163], v[52:55]\n" \
  "ds_read_b128 v[32:35], v8 offset:40960\n" \
  "v_mfma_f32_16x16x32_bf16 v[52:55], v[20:23], v[168:171], v[52:55]\n" \
  "v_mfma_f32_16x16x32_bf16 v[56:59], v[16:19], v[164:167], v[56:59]\n" \
  "ds_read_b128 v[16:19], v8 offset:47104\n" \
  "v_mfma_f32_16x16x32_bf16 v[56:59], v[20:23], v[172:175], v[56:59]\n" \
  "buffer_load_dwordx4 v[196:199], v8, s[64:67], s72 offen offset:1024 sc1\n" \
  "s_waitcnt lgkmcnt(2)\n" \
  "v_mfma_f32_16x16x32_bf16 v[68:71], v[24:27], v[160:163], v[68:71]\n" \
  "ds_read_b128 v[20:23], v8 offset:41984\n" \
  "v_mfma_f32_16x16x32_bf16 v[68:71], v[28:31], v[168:171], v[68:71]\n" \
  "v_mfma_f32_16x16x32_bf16 v[72:75], v[24:27], v[164:167], v[72:75]\n" \
  "ds_read_b128 v[24:27], v8 offset:48128\n" \
  "v_mfma_f32_16x16x32_bf16 v[72:75], v[28:31], v[172:175], v[72:75]\n" \
  "buffer_load_dwordx4 v[200:203], v8, s[64:67], s72 offen offset:2048 sc1\n" \
  "s_waitcnt lgkmcnt(2)\n" \
  "v_mfma_f32_16x16x32_bf16 v[76:79], v[32:35], v[160:163], v[76:79]\n" \
  "s_waitcnt vmcnt(7)\n" \
  "s_barrier\n" \
  "ds_read_b128 v[28:31], v4 offset:0\n" \
  "s_setprio 3\n" \
  "v_mfma_f32_16x16x32_bf16 v[76:79], v[16:19], v[168:171], v[76:79]\n" \
  "v_mfma_f32_16x16x32_bf16 v[80:83], v[32:35], v[164:167], v[80:83]\n" \
  "ds_read_b128 v[32:35], v4 offset:6144\n" \
  "v_mfma_f32_16x16x32_bf16 v[80:83], v[16:19], v[172:175], v[80:83]\n" \
  "buffer_load_dwordx4 v[204:207], v8, s[64:67], s72 offen offset:3072 sc1\n" \
  "s_waitcnt lgkmcnt(2)\n" \
  "v_mfma_f32_16x16x32_bf16 v[84:87], v[20:23], v[160:163], v[84:87]\n" \
  "ds_read_b128 v[16:19], v4 offset:1024\n" \
  "v_mfma_f32_16x16x32_bf16 v[84:87], v[24:27], v[168:171], v[84:87]\n" \
  "s_waitcnt vmcnt(6)\n" \
  "v_swap_b32 v178, v180\n" \
  "v_swap_b32 v179, v181\n" \
  "s_waitcnt vmcnt(4)\n" \
  "v_swap_b32 v186, v188\n" \
  "v_swap_b32 v187, v189\n" \
  "v_mfma_f32_16x16x32_bf16 v[88:91], v[20:23], v[164:167], v[88:91]\n" \
  "ds_read_b128 v[20:23], v4 offset:7168\n" \
  "v_mfma_f32_16x16x32_bf16 v[88:91], v[24:27], v[172:175], v[88:91]\n" \
  "s_waitcnt lgkmcnt(2)\n" \
  "v_mfma_f32_16x16x32_bf16 v[44:47], v[28:31], v[176:179], v[44:47]\n" \
  "ds_read_b128 v[24:27], v4 offset:2048\n" \
  "v_mfma_f32_16x16x32_bf16 v[44:47], v[32:35], v[184:187], v[44:47]\n" \
  "s_add_u32 s72, s72, 0x1000\n" \
  "buffer_load_dwordx4 v[208:211], v8, s[64:67], s72 offen offset:0 sc1\n" \
  "v_mfma_f32_16x16x32_bf16 v[48:51], v[28:31], v[180:183], v[48:51]\n" \
  "ds_read_b128 v[28:31], v4 offset:8192\n" \
  "v_mfma_f32_16x16x32_bf16 v[48:51], v[32:35], v[188:191], v[48:51]\n" \
  "s_waitcnt lgkmcnt(2)\n" \
  "v_mfma_f32_16x16x32_bf16 v[60:63], v[16:19], v[176:179], v[60:63]\n" \
  "ds_read_b128 v[32:35], v4 offset:3072\n" \
  "v_mfma_f32_16x16x32_bf16 v[60:63], v[20:23], v[184:187], v[60:63]\n" \
  "buffer_load_dwordx4 v[212:215], v8, s[64:67], s72 offen offset:1024 sc1\n" \
  "v_mfma_f32_16x16x32_bf16 v[64:67], v[16:19], v[180:183], v[64:67]\n" \
  "ds_read_b128 v[16:19], v4 offset:9216\n" \
  "v_mfma_f32_16x16x32_bf16 v[64:67], v[20:23], v[188:191], v[64:67]\n" \
  "s_waitcnt lgkmcnt(2)\n" \
  "v_mfma_f32_16x16x32_bf16 v[52:55], v[24:27], v[176:179], v[52:55]\n" \
  "ds_read_b128 v[20:23], v4 offset:4096\n" \
  "v_mfma_f32_16x16x32_bf16 v[52:55], v[28:31], v[184:187], v[52:55]\n" \
  "buffer_load_dwordx4 v[216:219], v8, s[64:67], s72 offen offset:2048 sc1\n" \
  "v_mfma_f32_16x16x32_bf16 v[56:59], v[24:27], v[180:183], v[56:59]\n" \
  "ds_read_b128 v[24:27], v4 offset:10240\n" \
  "v_mfma_f32_16x16x32_bf16 v[56:59], v[28:31], v[188:191], v[56:59]\n" \
  "s_waitcnt lgkmcnt(2)\n" \
  "v_mfma_f32_16x16x32_bf16 v[68:71], v[32:35], v[176:179], v[68:71]\n" \
  "ds_read_b128 v[28:31], v4 offset:5120\n" \
  "v_mfma_f32_16x16x32_bf16 v[68:71], v[16:19], v[184:187], v[68:71]\n" \
  "v_mfma_f32_16x16x32_bf16 v[72:75], v[32:35], v[180:183], v[72:75]\n" \
  "ds_read_b128 v[32:35], v4 offset:11264\n" \
  "v_mfma_f32_16x16x32_bf16 v[72:75], v[16:19], v[188:191], v[72:75]\n" \
  "buffer_load_dwordx4 v[220:223], v8, s[64:67], s72 offen offset:3072 sc1\n" \
  "s_waitcnt lgkmcnt(2)\n" \
  "v_mfma_f32_16x16x32_bf16 v[76:79], v[20:23], v[176:179], v[76:79]\n" \
  "ds_read_b128 v[16:19], v4 offset:12288\n" \
  "v_mfma_f32_16x16x32_bf16 v[76:79], v[24:27], v[184:187], v[76:79]\n" \
  "v_mfma_f32_16x16x32_bf16 v[80:83], v[20:23], v[180:183], v[80:83]\n" \
  "ds_read_b128 v[20:23], v4 offset:18432\n" \
  "v_mfma_f32_16x16x32_bf16 v[80:83], v[24:27], v[188:191], v[80:83]\n" \
  "s_add_u32 m0, s69, 90112\n" \
  "s_add_u32 s68, s68, 0x2000\n" \
  "buffer_load_dwordx4 v8, s[64:67], s68 offen sc1 lds\n" \
  "s_waitcnt lgkmcnt(2)\n" \
  "v_mfma_f32_16x16x32_bf16 v[84:87], v[28:31], v[176:179], v[84:87]\n" \
  "ds_read_b128 v[24:27], v4 offset:13312\n" \
  "v_mfma_f32_16x16x32_bf16 v[84:87], v[32:35], v[184:187], v[84:87]\n" \
  "s_waitcnt vmcnt(7)\n" \
  "v_swap_b32 v194, v196\n" \
  "v_swap_b32 v195, v197\n" \
  "s_waitcnt vmcnt(5)\n" \
  "v_swap_b32 v202, v204\n" \
  "v_swap_b32 v203, v205\n" \
  "v_mfma_f32_16x16x32_bf16 v[88:91], v[28:31], v[180:183], v[88:91]\n" \
  "ds_read_b128 v[28:31], v4 offset:19456\n" \
  "v_mfma_f32_16x16x32_bf16 v[88:91], v[32:35], v[188:191], v[88:91]\n" \
  "s_add_u32 m0, s69, 98304\n" \
  "s_add_u32 s68, s68, 0x2000\n" \
  "buffer_load_dwordx4 v8, s[64:67], s68 offen sc1 lds\n" \
  "s_waitcnt lgkmcnt(2)\n" \
  "v_mfma_f32_16x16x32_bf16 v[44:47], v[16:19], v[192:195], v[44:47]\n" \
  "ds_read_b128 v[32:35], v4 offset:14336\n" \
  "s_setprio 2\n" \
  "v_mfma_f32_16x16x32_bf16 v[44:47], v[20:23], v[200:203], v[44:47]\n" \
  "v_mfma_f32_16x16x32_bf16 v[48:51], v[16:19], v[196:199], v[48:51]\n" \
  "ds_read_b128 v[16:19], v4 offset:20480\n" \
  "v_mfma_f32_16x16x32_bf16 v[48:51], v[20:23], v[204:207], v[48:51]\n" \
  "s_waitcnt lgkmcnt(2)\n" \
  "v_mfma_f32_16x16x32_bf16 v[60:63], v[24:27], v[192:195], v[60:63]\n" \
  "ds_read_b128 v[20:23], v4 offset:15360\n" \
  "v_mfma_f32_16x16x32_bf16 v[60:63], v[28:31], v[200:203], v[60:63]\n" \
  "s_add_u32 m0, s69, 106496\n" \
  "s_add_u32 s68, s68, 0x2000\n" \
  "buffer_load_dwordx4 v8, s[64:67], s68 offen sc1 lds\n" \
  "v_mfma_f32_16x16x32_bf16 v[64:67], v[24:27], v[196:199], v[64:67]\n" \
  "ds_read_b128 v[24:27], v4 offset:21504\n" \
  "v_mfma_f32_16x16x32_bf16 v[64:67], v[28:31], v[204:207], v[64:67]\n" \
  "s_waitcnt lgkmcnt(2)\n" \
  "v_mfma_f32_16x16x32_bf16 v[52:55], v[32:35], v[192:195], v[52:55]\n" \
  "ds_read_b128 v[28:31], v4 offset:16384\n" \
  "v_mfma_f32_16x16x32_bf16 v[52:55], v[16:19], v[200:203], v[52:55]\n" \
  "s_add_u32 m0, s69, 114688\n" \
  "s_add_u32 s68, s68, 0x2000\n" \
  "buffer_load_dwordx4 v8, s[64:67], s68 offen sc1 lds\n" \
  "v_mfma_f32_16x16x32_bf16 v[56:59], v[32:35], v[196:199], v[56:59]\n" \
  "ds_read_b128 v[32:35], v4 offset:22528\n" \
  "v_mfma_f32_16x16x32_bf16 v[56:59], v[16:19], v[204:207], v[56:59]\n" \
  "s_waitcnt lgkmcnt(2)\n" \
  "v_mfma_f32_16x16x32_bf16 v[68:71], v[20:23], v[192:195], v[68:71]\n" \
  "ds_read_b128 v[16:19], v4 offset:17408\n" \
  "v_mfma_f32_16x16x32_bf16 v[68:71], v[24:27], v[200:203], v[68:71]\n" \
  "s_add_u32 s72, s72, 0x1000\n" \
  "buffer_load_dwordx4 v[224:227], v8, s[64:67], s72 offen offset:0 sc1\n" \
  "v_mfma_f32_16x16x32_bf16 v[72:75], v[20:23], v[196:199], v[72:75]\n" \
  "ds_read_b128 v[20:23], v4 offset:23552\n" \
  "v_mfma_f32_16x16x32_bf16 v[72:75], v[24:27], v[204:207], v[72:75]\n" \
  "s_waitcnt lgkmcnt(2)\n" \
  "v_mfma_f32_16x16x32_bf16 v[76:79], v[28:31], v[192:195], v[76:79]\n" \
  "ds_read_b128 v[24:27], v4 offset:24576\n" \
  "v_mfma_f32_16x16x32_bf16 v[76:79], v[32:35], v[200:203], v[76:79]\n" \
  "buffer_load_dwordx4 v[228:231], v8, s[64:67], s72 offen offset:1024 sc1\n" \
  "v_mfma_f32_16x16x32_bf16 v[80:83], v[28:31], v[196:199], v[80:83]\n" \
  "ds_read_b128 v[28:31], v4 offset:30720\n" \
  "v_mfma_f32_16x16x32_bf16 v[80:83], v[32:35], v[204:207], v[80:83]\n" \
  "s_waitcnt lgkmcnt(2)\n" \
  "v_mfma_f32_16x16x32_bf16 v[84:87], v[16:19], v[192:195], v[84:87]\n" \
  "ds_read_b128 v[32:35], v4 offset:25600\n" \
  "v_mfma_f32_16x16x32_bf16 v[84:87], v[20:23], v[200:203], v[84:87]\n" \
  "s_waitcnt vmcnt(8)\n" \
  "v_swap_b32 v210, v212\n" \
  "v_swap_b32 v211, v213\n" \
  "s_waitcnt vmcnt(6)\n" \
  "v_swap_b32 v218, v220\n" \
  "v_swap_b32 v219, v221\n" \
  "v_mfma_f32_16x16x32_bf16 v[88:91], v[16:19], v[196:199], v[88:91]\n" \
  "ds_read_b128 v[16:19], v4 offset:31744\n" \
  "s_setprio 1\n" \
  "v_mfma_f32_16x16x32_bf16 v[88:91], v[20:23], v[204:207], v[88:91]\n" \
  "buffer_load_dwordx4 v[232:235], v8, s[64:67], s72 offen offset:2048 sc1\n" \
  "s_waitcnt lgkmcnt(2)\n" \
  "v_mfma_f32_16x16x32_bf16 v[44:47], v[24:27], v[208:211], v[44:47]\n" \
  "ds_read_b128 v[20:23], v4 offset:26624\n" \
  "v_mfma_f32_16x16x32_bf16 v[44:47], v[28:31], v[216:219], v[44:47]\n" \
  "v_mfma_f32_16x16x32_bf16 v[48:51], v[24:27], v[212:215], v[48:51]\n" \
  "ds_read_b128 v[24:27], v4 offset:32768\n" \
  "v_mfma_f32_16x16x32_bf16 v[48:51], v[28:31], v[220:223], v[48:51]\n" \
  "buffer_load_dwordx4 v[236:239], v8, s[64:67], s72 offen offset:3072 sc1\n" \
  "s_waitcnt lgkmcnt(2)\n" \
  "v_mfma_f32_16x16x32_bf16 v[60:63], v[32:35], v[208:211], v[60:63]\n" \
  "ds_read_b128 v[28:31], v4 offset:27648\n" \
  "v_mfma_f32_16x16x32_bf16 v[60:63], v[16:19], v[216:219], v[60:63]\n" \
  "v_mfma_f32_16x16x32_bf16 v[64:67], v[32:35], v[212:215], v[64:67]\n" \
  "ds_read_b128 v[32:35], v4 offset:33792\n" \
  "v_mfma_f32_16x16x32_bf16 v[64:67], v[16:19], v[220:223], v[64:67]\n" \
  "s_add_u32 s72, s72, 0x1000\n" \
  "buffer_load_dwordx4 v[240:243], v8, s[64:67], s72 offen offset:0 sc1\n" \
  "s_waitcnt lgkmcnt(2)\n" \
  "v_mfma_f32_16x16x32_bf16 v[52:55], v[20:23], v[208:211], v[52:55]\n" \
  "ds_read_b128 v[16:19], v4 offset:28672\n" \
  "s_setprio 0\n" \
  "v_mfma_f32_16x16x32_bf16 v[52:55], v[24:27], v[216:219], v[52:55]\n" \
  "v_mfma_f32_16x16x32_bf16 v[56:59], v[20:23], v[212:215], v[56:59]\n" \
  "ds_read_b128 v[20:23], v4 offset:34816\n" \
  "v_mfma_f32_16x16x32_bf16 v[56:59], v[24:27], v[220:223], v[56:59]\n" \
  "s_waitcnt lgkmcnt(2)\n" \
  "v_mfma_f32_16x16x32_bf16 v[68:71], v[28:31], v[208:211], v[68:71]\n" \
  "ds_read_b128 v[24:27], v4 offset:29696\n" \
  "v_mfma_f32_16x16x32_bf16 v[68:71], v[32:35], v[216:219], v[68:71]\n" \
  "buffer_load_dwordx4 v[244:247], v8, s[64:67], s72 offen offset:1024 sc1\n" \
  "v_mfma_f32_16x16x32_bf16 v[72:75], v[28:31], v[212:215], v[72:75]\n" \
  "ds_read_b128 v[28:31], v4 offset:35840\n" \
  "v_mfma_f32_16x16x32_bf16 v[72:75], v[32:35], v[220:223], v[72:75]\n" \
  "s_waitcnt lgkmcnt(2)\n" \
  "v_mfma_f32_16x16x32_bf16 v[76:79], v[16:19], v[208:211], v[76:79]\n" \
  "s_waitcnt vmcnt(6)\n" \
  "s_barrier\n" \
  "ds_read_b128 v[32:35], v4 offset:36864\n" \
  "s_setprio 3\n" \
  "v_mfma_f32_16x16x32_bf16 v[76:79], v[20:23], v[216:219], v[76:79]\n" \
  "buffer_load_dwordx4 v[248:251], v8, s[64:67], s72 offen offset:2048 sc1\n" \
  "v_mfma_f32_16x16x32_bf16 v[80:83], v[16:19], v[212:215], v[80:83]\n" \
  "ds_read_b128 v[16:19], v4 offset:43008\n" \
  "v_mfma_f32_16x16x32_bf16 v[80:83], v[20:23], v[220:223], v[80:83]\n" \
  "s_waitcnt lgkmcnt(2)\n" \
  "v_mfma_f32_16x16x32_bf16 v[84:87], v[24:27], v[208:211], v[84:87]\n" \
  "ds_read_b128 v[20:23], v4 offset:37888\n" \
  "v_mfma_f32_16x16x32_bf16 v[84:87], v[28:31], v[216:219], v[84:87]\n" \
  "buffer_load_dwordx4 v[252:255], v8, s[64:67], s72 offen offset:3072 sc1\n" \
  "s_waitcnt vmcnt(6)\n" \
  "v_swap_b32 v226, v228\n" \
  "v_swap_b32 v227, v229\n" \
  "s_waitcnt vmcnt(4)\n" \
  "v_swap_b32 v234, v236\n" \
  "v_swap_b32 v235, v237\n" \
  "v_mfma_f32_16x16x32_bf16 v[88:91], v[24:27], v[212:215], v[88:91]\n" \
  "ds_read_b128 v[24:27], v4 offset:44032\n" \
  "v_mfma_f32_16x16x32_bf16 v[88:91], v[28:31], v[220:223], v[88:91]\n" \
  "s_waitcnt lgkmcnt(2)\n" \
  "v_mfma_f32_16x16x32_bf16 v[44:47], v[32:35], v[224:227], v[44:47]\n" \
  "ds_read_b128 v[28:31], v4 offset:38912\n" \
  "v_mfma_f32_16x16x32_bf16 v[44:47], v[16:19], v[232:235], v[44:47]\n" \
  "v_mfma_f32_16x16x32_bf16 v[48:51], v[32:35], v[228:231], v[48:51]\n" \
  "ds_read_b128 v[32:35], v4 offset:45056\n" \
  "v_mfma_f32_16x16x32_bf16 v[48:51], v[16:19], v[236:239], v[48:51]\n" \
  "s_add_u32 m0, s69, 122880\n" \
  "s_add_u32 s68, s68, 0x2000\n" \
  "buffer_load_dwordx4 v8, s[64:67], s68 offen sc1 lds\n" \
  "s_waitcnt lgkmcnt(2)\n" \
  "v_mfma_f32_16x16x32_bf16 v[60:63], v[20:23], v[224:227], v[60:63]\n" \
  "ds_read_b128 v[16:19], v4 offset:39936\n" \
  "v_mfma_f32_16x16x32_bf16 v[60:63], v[24:27], v[232:235], v[60:63]\n" \
  "v_mfma_f32_16x16x32_bf16 v[64:67], v[20:23], v[228:231], v[64:67]\n" \
  "ds_read_b128 v[20:23], v4 offset:46080\n" \
  "v_mfma_f32_16x16x32_bf16 v[64:67], v[24:27], v[236:239], v[64:67]\n" \
  "s_add_u32 m0, s69, 131072\n" \
  "s_add_u32 s68, s68, 0x2000\n" \
  "buffer_load_dwordx4 v8, s[64:67], s68 offen sc1 lds\n" \
  "s_waitcnt lgkmcnt(2)\n" \
  "v_mfma_f32_16x16x32_bf16 v[52:55], v[28:31], v[224:227], v[52:55]\n" \
  "ds_read_b128 v[24:27], v4 offset:40960\n" \
  "v_mfma_f32_16x16x32_bf16 v[52:55], v[32:35], v[232:235], v[52:55]\n" \
  "v_mfma_f32_16x16x32_bf16 v[56:59], v[28:31], v[228:231], v[56:59]\n" \
  "ds_read_b128 v[28:31], v4 offset:47104\n" \
  "v_mfma_f32_16x16x32_bf16 v[56:59], v[32:35], v[236:239], v[56:59]\n" \
  "s_add_u32 m0, s69, 139264\n" \
  "s_add_u32 s68, s68, 0x2000\n" \
  "buffer_load_dwordx4 v8, s[64:67], s68 offen sc1 lds\n" \
  "s_waitcnt lgkmcnt(2)\n" \
  "v_mfma_f32_16x16x32_bf16 v[68:71], v[16:19], v[224:227], v[68:71]\n" \
  "ds_read_b128 v[32:35], v4 offset:41984\n" \
  "v_mfma_f32_16x16x32_bf16 v[68:71], v[20:23], v[232:235], v[68:71]\n" \
  "v_mfma_f32_16x16x32_bf16 v[72:75], v[16:19], v[228:231], v[72:75]\n" \
  "ds_read_b128 v[16:19], v4 offset:48128\n" \
  "v_mfma_f32_16x16x32_bf16 v[72:75], v[20:23], v[236:239], v[72:75]\n" \
  "s_add_u32 m0, s69, 0\n" \
  "s_add_u32 s68, s68, 0x2000\n" \
  "buffer_load_dwordx4 v8, s[64:67], s68 offen sc1 lds\n" \
  "s_waitcnt lgkmcnt(2)\n" \
  "v_mfma_f32_16x16x32_bf16 v[76:79], v[24:27], v[224:227], v[76:79]\n" \
  "ds_read_b128 v[20:23], v5 offset:0\n" \
  "v_mfma_f32_16x16x32_bf16 v[76:79], v[28:31], v[232:235], v[76:79]\n" \
  "v_mfma_f32_16x16x32_bf16 v[80:83], v[24:27], v[228:231], v[80:83]\n" \
  "ds_read_b128 v[24:27], v5 offset:6144\n" \
  "v_mfma_f32_16x16x32_bf16 v[80:83], v[28:31], v[236:239], v[80:83]\n" \
  "s_waitcnt lgkmcnt(2)\n" \
  "v_mfma_f32_16x16x32_bf16 v[84:87], v[32:35], v[224:227], v[84:87]\n" \
  "ds_read_b128 v[28:31], v5 offset:1024\n" \
  "v_mfma_f32_16x16x32_bf16 v[84:87], v[16:19], v[232:235], v[84:87]\n" \
  "s_add_u32 m0, s69, 8192\n" \
  "s_add_u32 s68, s68, 0x2000\n" \
  "buffer_load_dwordx4 v8, s[64:67], s68 offen sc1 lds\n" \
  "s_waitcnt vmcnt(7)\n" \
  "v_swap_b32 v242, v244\n" \
  "v_swap_b32 v243, v245\n" \
  "s_waitcnt vmcnt(5)\n" \
  "v_swap_b32 v250, v252\n" \
  "v_swap_b32 v251, v253\n" \
  "v_mfma_f32_16x16x32_bf16 v[88:91], v[32:35], v[228:231], v[88:91]\n" \
  "ds_read_b128 v[32:35], v5 offset:7168\n" \
  "v_mfma_f32_16x16x32_bf16 v[88:91], v[16:19], v[236:239], v[88:91]\n" \
  "s_waitcnt lgkmcnt(2)\n" \
  "v_mfma_f32_16x16x32_bf16 v[44:47], v[20:23], v[240:243], v[44:47]\n" \
  "ds_read_b128 v[16:19], v5 offset:2048\n" \
  "s_setprio 2\n" \
  "v_mfma_f32_16x16x32_bf16 v[44:47], v[24:27], v[248:251], v[44:47]\n" \
  "s_add_u32 m0, s69, 16384\n" \
  "s_add_u32 s68, s68, 0x2000\n" \
  "buffer_load_dwordx4 v8, s[64:67], s68 offen sc1 lds\n" \
  "v_mfma_f32_16x16x32_bf16 v[48:51], v[20:23], v[244:247], v[48:51]\n" \
  "ds_read_b128 v[20:23], v5 offset:8192\n" \
  "v_mfma_f32_16x16x32_bf16 v[48:51], v[24:27], v[252:255], v[48:51]\n" \
  "s_waitcnt lgkmcnt(2)\n" \
  "v_mfma_f32_16x16x32_bf16 v[60:63], v[28:31], v[240:243], v[60:63]\n" \
  "ds_read_b128 v[24:27], v5 offset:3072\n" \
  "v_mfma_f32_16x16x32_bf16 v[60:63], v[32:35], v[248:251], v[60:63]\n" \
  "s_add_u32 m0, s69, 24576\n" \
  "s_add_u32 s68, s68, 0x2000\n" \
  "buffer_load_dwordx4 v8, s[64:67], s68 offen sc1 lds\n" \
  "v_mfma_f32_16x16x32_bf16 v[64:67], v[28:31], v[244:247], v[64:67]\n" \
  "ds_read_b128 v[28:31], v5 offset:9216\n" \
  "v_mfma_f32_16x16x32_bf16 v[64:67], v[32:35], v[252:255], v[64:67]\n" \
  "s_waitcnt lgkmcnt(2)\n" \
  "v_mfma_f32_16x16x32_bf16 v[52:55], v[16:19], v[240:243], v[52:55]\n" \
  "ds_read_b128 v[32:35], v5 offset:4096\n" \
  "v_mfma_f32_16x16x32_bf16 v[52:55], v[20:23], v[248:251], v[52:55]\n" \
  "v_mfma_f32_16x16x32_bf16 v[56:59], v[16:19], v[244:247], v[56:59]\n" \
  "ds_read_b128 v[16:19], v5 offset:10240\n" \
  "v_mfma_f32_16x16x32_bf16 v[56:59], v[20:23], v[252:255], v[56:59]\n" \
  "s_add_u32 m0, s69, 32768\n" \
  "s_add_u32 s68, s68, 0x2000\n" \
  "buffer_load_dwordx4 v8, s[64:67], s68 offen sc1 lds\n" \
  "s_waitcnt lgkmcnt(2)\n" \
  "v_mfma_f32_16x16x32_bf16 v[68:71], v[24:27], v[240:243], v[68:71]\n" \
  "ds_read_b128 v[20:23], v5 offset:5120\n" \
  "v_mfma_f32_16x16x32_bf16 v[68:71], v[28:31], v[248:251], v[68:71]\n" \
  "v_mfma_f32_16x16x32_bf16 v[72:75], v[24:27], v[244:247], v[72:75]\n" \
  "ds_read_b128 v[24:27], v5 offset:11264\n" \
  "v_mfma_f32_16x16x32_bf16 v[72:75], v[28:31], v[252:255], v[72:75]\n" \
  "s_add_u32 m0, s69, 40960\n" \
  "s_add_u32 s68, s68, 0x2000\n" \
  "buffer_load_dwordx4 v8, s[64:67], s68 offen sc1 lds\n" \
  "s_waitcnt lgkmcnt(2)\n" \
  "v_mfma_f32_16x16x32_bf16 v[76:79], v[32:35], v[240:243], v[76:79]\n" \
  "ds_read_b128 v[28:31], v5 offset:12288\n" \
  "v_mfma_f32_16x16x32_bf16 v[76:79], v[16:19], v[248:251], v[76:79]\n" \
  "v_mfma_f32_16x16x32_bf16 v[80:83], v[32:35], v[244:247], v[80:83]\n" \
  "ds_read_b128 v[32:35], v5 offset:13312\n" \
  "v_mfma_f32_16x16x32_bf16 v[80:83], v[16:19], v[252:255], v[80:83]\n" \
  "s_add_u32 m0, s69, 49152\n" \
  "s_add_u32 s68, s68, 0x2000\n" \
  "buffer_load_dwordx4 v8, s[64:67], s68 offen sc1 lds\n" \
  "s_waitcnt lgkmcnt(2)\n" \
  "v_mfma_f32_16x16x32_bf16 v[84:87], v[20:23], v[240:243], v[84:87]\n" \
  "ds_read_b128 v[16:19], v5 offset:14336\n" \
  "v_mfma_f32_16x16x32_bf16 v[84:87], v[24:27], v[248:251], v[84:87]\n" \
  "v_mfma_f32_16x16x32_bf16 v[88:91], v[20:23], v[244:247], v[88:91]\n" \
  "ds_read_b128 v[20:23], v5 offset:15360\n" \
  "s_setprio 1\n" \
  "v_mfma_f32_16x16x32_bf16 v[88:91], v[24:27], v[252:255], v[88:91]\n" \
  "s_waitcnt lgkmcnt(2)\n" \
  "v_mfma_f32_16x16x32_bf16 v[92:95], v[28:31], v[112:115], 0\n" \
  "ds_read_b128 v[24:27], v5 offset:16384\n" \
  "v_mfma_f32_16x16x32_bf16 v[92:95], v[32:35], v[120:123], v[92:95]\n" \
  "s_add_u32 m0, s69, 57344\n" \
  "s_add_u32 s68, s68, 0x2000\n" \
  "buffer_load_dwordx4 v8, s[64:67], s68 offen sc1 lds\n" \
  "v_mfma_f32_16x16x32_bf16 v[96:99], v[28:31], v[116:119], 0\n" \
  "ds_read_b128 v[28:31], v5 offset:17408\n" \
  "v_mfma_f32_16x16x32_bf16 v[96:99], v[32:35], v[124:127], v[96:99]\n" \
  "s_waitcnt lgkmcnt(2)\n" \
  "v_mfma_f32_16x16x32_bf16 v[96:99], v[16:19], v[132:135], v[96:99]\n" \
  "ds_read_b128 v[32:35], v5 offset:18432\n" \
  "v_mfma_f32_16x16x32_bf16 v[96:99], v[20:23], v[140:143], v[96:99]\n" \
  "v_mfma_f32_16x16x32_bf16 v[92:95], v[16:19], v[128:131], v[92:95]\n" \
  "ds_read_b128 v[16:19], v5 offset:19456\n" \
  "v_mul_f32 v76, v76, v84\n" \
  "v_mfma_f32_16x16x32_bf16 v[92:95], v[20:23], v[136:139], v[92:95]\n" \
  "v_mul_f32 v77, v77, v85\n" \
  "s_add_u32 m0, s69, 65536\n" \
  "s_add_u32 s68, s68, 0x2000\n" \
  "buffer_load_dwordx4 v8, s[64:67], s68 offen sc1 lds\n" \
  "s_waitcnt lgkmcnt(2)\n" \
  "v_mfma_f32_16x16x32_bf16 v[92:95], v[24:27], v[144:147], v[92:95]\n" \
  "ds_read_b128 v[20:23], v5 offset:20480\n" \
  "s_setprio 0\n" \
  "v_mul_f32 v78, v78, v86\n" \
  "v_mfma_f32_16x16x32_bf16 v[92:95], v[28:31], v[152:155], v[92:95]\n" \
  "v_mul_f32 v79, v79, v87\n" \
  "v_mfma_f32_16x16x32_bf16 v[96:99], v[24:27], v[148:151], v[96:99]\n" \
  "ds_read_b128 v[24:27], v5 offset:21504\n" \
  "v_cvt_pk_bf16_f32 v36, v76, v77\n" \
  "v_mfma_f32_16x16x32_bf16 v[96:99], v[28:31], v[156:159], v[96:99]\n" \
  "v_cvt_pk_bf16_f32 v37, v78, v79\n" \
  "s_waitcnt lgkmcnt(2)\n" \
  "v_mfma_f32_16x16x32_bf16 v[96:99], v[32:35], v[164:167], v[96:99]\n" \
  "ds_read_b128 v[28:31], v5 offset:22528\n" \
  "v_mul_f32 v80, v80, v88\n" \
  "v_mfma_f32_16x16x32_bf16 v[96:99], v[16:19], v[172:175], v[96:99]\n" \
  "v_mul_f32 v81, v81, v89\n" \
  "v_mfma_f32_16x16x32_bf16 v[92:95], v[32:35], v[160:163], v[92:95]\n" \
  "ds_read_b128 v[32:35], v5 offset:23552\n" \
  "v_mul_f32 v82, v82, v90\n" \
  "v_mfma_f32_16x16x32_bf16 v[92:95], v[16:19], v[168:171], v[92:95]\n" \
  "v_mul_f32 v83, v83, v91\n" \
  "s_waitcnt lgkmcnt(2)\n" \
  "v_mfma_f32_16x16x32_bf16 v[92:95], v[20:23], v[176:179], v[92:95]\n" \
  "s_waitcnt vmcnt(6)\n" \
  "s_barrier\n" \
  "ds_read_b128 v[16:19], v5 offset:24576\n" \
  "s_setprio 3\n" \
  "v_cvt_pk_bf16_f32 v38, v80, v81\n" \
  "v_mfma_f32_16x16x32_bf16 v[92:95], v[24:27], v[184:187], v[92:95]\n" \
  "v_cvt_pk_bf16_f32 v39, v82, v83\n" \
  "s_add_u32 m0, s69, 73728\n" \
  "s_add_u32 s68, s68, 0x2000\n" \
  "buffer_load_dwordx4 v8, s[64:67], s68 offen sc1 lds\n" \
  "v_mfma_f32_16x16x32_bf16 v[96:99], v[20:23], v[180:183], v[96:99]\n" \
  "ds_read_b128 v[20:23], v5 offset:25600\n" \
  "s_mov_b64 exec, s[80:81]\n" \
  "v_swap_b32 v36, v38\n" \
  "v_swap_b32 v37, v39\n" \
  "s_mov_b64 exec, -1\n" \
  "v_mfma_f32_16x16x32_bf16 v[96:99], v[24:27], v[188:191], v[96:99]\n" \
  "buffer_store_dwordx4 v[36:39], v8, s[64:67], s75 offen sc1\n" \
  "s_waitcnt lgkmcnt(2)\n" \
  "v_mfma_f32_16x16x32_bf16 v[96:99], v[28:31], v[196:199], v[96:99]\n" \
  "ds_read_b128 v[24:27], v5 offset:26624\n" \
  "v_mfma_f32_16x16x32_bf16 v[96:99], v[32:35], v[204:207], v[96:99]\n" \
  "s_add_u32 m0, s69, 81920\n" \
  "s_add_u32 s68, s68, 0x2000\n" \
  "buffer_load_dwordx4 v8, s[64:67], s68 offen sc1 lds\n" \
  "v_mfma_f32_16x16x32_bf16 v[92:95], v[28:31], v[192:195], v[92:95]\n" \
  "ds_read_b128 v[28:31], v5 offset:27648\n" \
  "v_mul_f32 v44, v44, v52\n" \
  "v_mfma_f32_16x16x32_bf16 v[92:95], v[32:35], v[200:203], v[92:95]\n" \
  "v_mul_f32 v45, v45, v53\n" \
  "s_waitcnt lgkmcnt(2)\n" \
  "v_mfma_f32_16x16x32_bf16 v[92:95], v[16:19], v[208:211], v[92:95]\n" \
  "ds_read_b128 v[32:35], v5 offset:28672\n" \
  "v_mul_f32 v46, v46, v54\n" \
  "v_mfma_f32_16x16x32_bf16 v[92:95], v[20:23], v[216:219], v[92:95]\n" \
  "v_mul_f32 v47, v47, v55\n" \
  "v_mfma_f32_16x16x32_bf16 v[96:99], v[16:19], v[212:215], v[96:99]\n" \
  "ds_read_b128 v[16:19], v5 offset:29696\n" \
  "v_cvt_pk_bf16_f32 v36, v44, v45\n" \
  "v_mfma_f32_16x16x32_bf16 v[96:99], v[20:23], v[220:223], v[96:99]\n" \
  "v_cvt_pk_bf16_f32 v37, v46, v47\n" \
  "s_add_u32 m0, s69, 90112\n" \
  "s_add_u32 s68, s68, 0x2000\n" \
  "buffer_load_dwordx4 v8, s[64:67], s68 offen sc1 lds\n" \
  "s_waitcnt lgkmcnt(2)\n" \
  "v_mfma_f32_16x16x32_bf16 v[96:99], v[24:27], v[228:231], v[96:99]\n" \
  "ds_read_b128 v[20:23], v5 offset:30720\n" \
  "v_mul_f32 v48, v48, v56\n" \
  "v_mfma_f32_16x16x32_bf16 v[96:99], v[28:31], v[236:239], v[96:99]\n" \
  "v_mul_f32 v49, v49, v57\n" \
  "v_mfma_f32_16x16x32_bf16 v[92:95], v[24:27], v[224:227], v[92:95]\n" \
  "ds_read_b128 v[24:27], v5 offset:31744\n" \
  "v_mul_f32 v50, v50, v58\n" \
  "v_mfma_f32_16x16x32_bf16 v[92:95], v[28:31], v[232:235], v[92:95]\n" \
  "v_mul_f32 v51, v51, v59\n" \
  "s_waitcnt lgkmcnt(2)\n" \
  "v_mfma_f32_16x16x32_bf16 v[92:95], v[32:35], v[240:243], v[92:95]\n" \
  "ds_read_b128 v[28:31], v5 offset:32768\n" \
  "v_cvt_pk_bf16_f32 v40, v48, v49\n" \
  "v_mfma_f32_16x16x32_bf16 v[92:95], v[16:19], v[248:251], v[92:95]\n" \
  "v_cvt_pk_bf16_f32 v41, v50, v51\n" \
  "s_add_u32 m0, s69, 98304\n" \
  "s_add_u32 s68, s68, 0x2000\n" \
  "buffer_load_dwordx4 v8, s[64:67], s68 offen sc1 lds\n" \
  "v_mfma_f32_16x16x32_bf16 v[96:99], v[32:35], v[244:247], v[96:99]\n" \
  "ds_read_b128 v[32:35], v5 offset:33792\n" \
  "v_mul_f32 v60, v60, v68\n" \
  "v_mfma_f32_16x16x32_bf16 v[96:99], v[16:19], v[252:255], v[96:99]\n" \
  "v_mul_f32 v61, v61, v69\n" \
  "s_waitcnt lgkmcnt(2)\n" \
  "v_mfma_f32_16x16x32_bf16 v[100:103], v[20:23], v[112:115], 0\n" \
  "ds_read_b128 v[16:19], v5 offset:34816\n" \
  "v_mul_f32 v62, v62, v70\n" \
  "v_mfma_f32_16x16x32_bf16 v[100:103], v[24:27], v[120:123], v[100:103]\n" \
  "v_mul_f32 v63, v63, v71\n" \
  "v_mfma_f32_16x16x32_bf16 v[104:107], v[20:23], v[116:119], 0\n" \
  "ds_read_b128 v[20:23], v5 offset:35840\n" \
  "v_cvt_pk_bf16_f32 v38, v60, v61\n" \
  "v_mfma_f32_16x16x32_bf16 v[104:107], v[24:27], v[124:127], v[104:107]\n" \
  "v_cvt_pk_bf16_f32 v39, v62, v63\n" \
  "s_add_u32 m0, s69, 106496\n" \
  "s_add_u32 s68, s68, 0x2000\n" \
  "buffer_load_dwordx4 v8, s[64:67], s68 offen sc1 lds\n" \
  "s_waitcnt lgkmcnt(2)\n" \
  "v_mfma_f32_16x16x32_bf16 v[104:107], v[28:31], v[132:135], v[104:107]\n" \
  "ds_read_b128 v[24:27], v5 offset:36864\n" \
  "v_mul_f32 v64, v64, v72\n" \
  "v_mfma_f32_16x16x32_bf16 v[104:107], v[32:35], v[140:143], v[104:107]\n" \
  "v_mul_f32 v65, v65, v73\n" \
  "v_mfma_f32_16x16x32_bf16 v[100:103], v[28:31], v[128:131], v[100:103]\n" \
  "ds_read_b128 v[28:31], v5 offset:37888\n" \
  "v_mul_f32 v66, v66, v74\n" \
  "v_mfma_f32_16x16x32_bf16 v[100:103], v[32:35], v[136:139], v[100:103]\n" \
  "v_mul_f32 v67, v67, v75\n" \
  "s_waitcnt lgkmcnt(2)\n" \
  "v_mfma_f32_16x16x32_bf16 v[100:103], v[16:19], v[144:147], v[100:103]\n" \
  "ds_read_b128 v[32:35], v5 offset:38912\n" \
  "v_cvt_pk_bf16_f32 v42, v64, v65\n" \
  "v_mfma_f32_16x16x32_bf16 v[100:103], v[20:23], v[152:155], v[100:103]\n" \
  "v_cvt_pk_bf16_f32 v43, v66, v67\n" \
  "v_mfma_f32_16x16x32_bf16 v[104:107], v[16:19], v[148:151], v[104:107]\n" \
  "ds_read_b128 v[16:19], v5 offset:39936\n" \
  "buffer_store_dwordx4 v[36:39], v8, s[64:67], s74 offen offset:0 sc1\n" \
  "v_mfma_f32_16x16x32_bf16 v[104:107], v[20:23], v[156:159], v[104:107]\n" \
  "buffer_store_dwordx4 v[40:43], v8, s[64:67], s74 offen offset:1024 sc1\n" \
  "s_waitcnt lgkmcnt(2)\n" \
  "v_mfma_f32_16x16x32_bf16 v[104:107], v[24:27], v[164:167], v[104:107]\n" \
  "ds_read_b128 v[20:23], v5 offset:40960\n" \
  "v_mfma_f32_16x16x32_bf16 v[104:107], v[28:31], v[172:175], v[104:107]\n" \
  "v_mfma_f32_16x16x32_bf16 v[100:103], v[24:27], v[160:163], v[100:103]\n" \
  "ds_read_b128 v[24:27], v5 offset:41984\n" \
  "v_mfma_f32_16x16x32_bf16 v[100:103], v[28:31], v[168:171], v[100:103]\n" \
  "s_waitcnt lgkmcnt(2)\n" \
  "v_mfma_f32_16x16x32_bf16 v[100:103], v[32:35], v[176:179], v[100:103]\n" \
  "ds_read_b128 v[28:31], v5 offset:43008\n" \
  "v_mfma_f32_16x16x32_bf16 v[100:103], v[16:19], v[184:187], v[100:103]\n" \
  "v_mfma_f32_16x16x32_bf16 v[104:107], v[32:35], v[180:183], v[104:107]\n" \
  "ds_read_b128 v[32:35], v5 offset:44032\n" \
  "v_mfma_f32_16x16x32_bf16 v[104:107], v[16:19], v[188:191], v[104:107]\n" \
  "s_waitcnt lgkmcnt(2)\n" \
  "v_mfma_f32_16x16x32_bf16 v[104:107], v[20:23], v[196:199], v[104:107]\n" \
  "ds_read_b128 v[16:19], v5 offset:45056\n" \
  "v_mfma_f32_16x16x32_bf16 v[104:107], v[24:27], v[204:207], v[104:107]\n" \
  "v_mfma_f32_16x16x32_bf16 v[100:103], v[20:23], v[192:195], v[100:103]\n" \
  "ds_read_b128 v[20:23], v5 offset:46080\n" \
  "s_setprio 2\n" \
  "v_mfma_f32_16x16x32_bf16 v[100:103], v[24:27], v[200:203], v[100:103]\n" \
  "s_waitcnt lgkmcnt(2)\n" \
  "v_mfma_f32_16x16x32_bf16 v[100:103], v[28:31], v[208:211], v[100:103]\n" \
  "ds_read_b128 v[24:27], v5 offset:47104\n" \
  "v_mfma_f32_16x16x32_bf16 v[100:103], v[32:35], v[216:219], v[100:103]\n" \
  "v_mfma_f32_16x16x32_bf16 v[104:107], v[28:31], v[212:215], v[104:107]\n" \
  "ds_read_b128 v[28:31], v5 offset:48128\n" \
  "v_mfma_f32_16x16x32_bf16 v[104:107], v[32:35], v[220:223], v[104:107]\n" \
  "s_waitcnt lgkmcnt(2)\n" \
  "v_mfma_f32_16x16x32_bf16 v[104:107], v[16:19], v[228:231], v[104:107]\n" \
  "ds_read_b128 v[32:35], v8 offset:0\n" \
  "v_mfma_f32_16x16x32_bf16 v[104:107], v[20:23], v[236:239], v[104:107]\n" \
  "v_mfma_f32_16x16x32_bf16 v[100:103], v[16:19], v[224:227], v[100:103]\n" \
  "ds_read_b128 v[16:19], v8 offset:1024\n" \
  "v_mfma_f32_16x16x32_bf16 v[100:103], v[20:23], v[232:235], v[100:103]\n" \
  "s_waitcnt lgkmcnt(2)\n" \
  "v_mfma_f32_16x16x32_bf16 v[100:103], v[24:27], v[240:243], v[100:103]\n" \
  "ds_read_b128 v[20:23], v8 offset:2048\n" \
  "v_mfma_f32_16x16x32_bf16 v[100:103], v[28:31], v[248:251], v[100:103]\n" \
  "v_mfma_f32_16x16x32_bf16 v[104:107], v[24:27], v[244:247], v[104:107]\n" \
  "ds_read_b128 v[24:27], v8 offset:3072\n" \
  "v_mfma_f32_16x16x32_bf16 v[104:107], v[28:31], v[252:255], v[104:107]\n" \
  "s_waitcnt lgkmcnt(2)\n" \
  "v_mfma_f32_16x16x32_bf16 v[44:47], v[32:35], v[112:115], 0\n" \
  "ds_read_b128 v[28:31], v8 offset:4096\n" \
  "v_mfma_f32_16x16x32_bf16 v[44:47], v[16:19], v[120:123], v[44:47]\n" \
  "v_mfma_f32_16x16x32_bf16 v[48:51], v[32:35], v[116:119], 0\n" \
  "ds_read_b128 v[32:35], v8 offset:5120\n" \
  "v_mfma_f32_16x16x32_bf16 v[48:51], v[16:19], v[124:127], v[48:51]\n" \
  "s_waitcnt lgkmcnt(2)\n" \
  "v_mfma_f32_16x16x32_bf16 v[48:51], v[20:23], v[132:135], v[48:51]\n" \
  "ds_read_b128 v[16:19], v8 offset:6144\n" \
  "v_mfma_f32_16x16x32_bf16 v[48:51], v[24:27], v[140:143], v[48:51]\n" \
  "buffer_load_dword v76, v15, s[64:67], s84 offen sc1\n" \
  "s_add_u32 s84, s84, 0x8000\n" \
  "v_mfma_f32_16x16x32_bf16 v[44:47], v[20:23], v[128:131], v[44:47]\n" \
  "ds_read_b128 v[20:23], v8 offset:7168\n" \
  "v_mul_f32 v92, v92, v100\n" \
  "v_mfma_f32_16x16x32_bf16 v[44:47], v[24:27], v[136:139], v[44:47]\n" \
  "v_mul_f32 v93, v93, v101\n" \
  "s_waitcnt lgkmcnt(2)\n" \
  "v_mfma_f32_16x16x32_bf16 v[44:47], v[28:31], v[144:147], v[44:47]\n" \
  "ds_read_b128 v[24:27], v8 offset:8192\n" \
  "v_mul_f32 v94, v94, v102\n" \
  "v_mfma_f32_16x16x32_bf16 v[44:47], v[32:35], v[152:155], v[44:47]\n" \
  "v_mul_f32 v95, v95, v103\n" \
  "buffer_load_dword v77, v15, s[64:67], s84 offen sc1\n" \
  "s_add_u32 s84, s84, 0x8000\n" \
  "v_mfma_f32_16x16x32_bf16 v[48:51], v[28:31], v[148:151], v[48:51]\n" \
  "ds_read_b128 v[28:31], v8 offset:9216\n" \
  "v_cvt_pk_bf16_f32 v36, v92, v93\n" \
  "v_mfma_f32_16x16x32_bf16 v[48:51], v[32:35], v[156:159], v[48:51]\n" \
  "v_cvt_pk_bf16_f32 v37, v94, v95\n" \
  "s_waitcnt lgkmcnt(2)\n" \
  "v_mfma_f32_16x16x32_bf16 v[48:51], v[16:19], v[164:167], v[48:51]\n" \
  "ds_read_b128 v[32:35], v8 offset:10240\n" \
  "v_mul_f32 v96, v96, v104\n" \
  "v_mfma_f32_16x16x32_bf16 v[48:51], v[20:23], v[172:175], v[48:51]\n" \
  "v_mul_f32 v97, v97, v105\n" \
  "v_mfma_f32_16x16x32_bf16 v[44:47], v[16:19], v[160:163], v[44:47]\n" \
  "ds_read_b128 v[16:19], v8 offset:11264\n" \
  "v_mul_f32 v98, v98, v106\n" \
  "v_mfma_f32_16x16x32_bf16 v[44:47], v[20:23], v[168:171], v[44:47]\n" \
  "v_mul_f32 v99, v99, v107\n" \
  "buffer_load_dword v78, v15, s[64:67], s84 offen sc1\n" \
  "s_add_u32 s84, s84, 0x8000\n" \
  "s_waitcnt lgkmcnt(2)\n" \
  "v_mfma_f32_16x16x32_bf16 v[44:47], v[24:27], v[176:179], v[44:47]\n" \
  "ds_read_b128 v[20:23], v8 offset:12288\n" \
  "s_setprio 1\n" \
  "v_cvt_pk_bf16_f32 v40, v96, v97\n" \
  "v_mfma_f32_16x16x32_bf16 v[44:47], v[28:31], v[184:187], v[44:47]\n" \
  "v_cvt_pk_bf16_f32 v41, v98, v99\n" \
  "v_mfma_f32_16x16x32_bf16 v[48:51], v[24:27], v[180:183], v[48:51]\n" \
  "ds_read_b128 v[24:27], v8 offset:13312\n" \
  "v_mfma_f32_16x16x32_bf16 v[48:51], v[28:31], v[188:191], v[48:51]\n" \
  "s_waitcnt lgkmcnt(2)\n" \
  "v_mfma_f32_16x16x32_bf16 v[48:51], v[32:35], v[196:199], v[48:51]\n" \
  "ds_read_b128 v[28:31], v8 offset:14336\n" \
  "v_mfma_f32_16x16x32_bf16 v[48:51], v[16:19], v[204:207], v[48:51]\n" \
  "buffer_load_dword v79, v15, s[64:67], s84 offen sc1\n" \
  "s_add_u32 s84, s84, 0x8000\n" \
  "v_mfma_f32_16x16x32_bf16 v[44:47], v[32:35], v[192:195], v[44:47]\n" \
  "ds_read_b128 v[32:35], v8 offset:15360\n" \
  "v_mfma_f32_16x16x32_bf16 v[44:47], v[16:19], v[200:203], v[44:47]\n" \
  "s_waitcnt lgkmcnt(2)\n" \
  "v_mfma_f32_16x16x32_bf16 v[44:47], v[20:23], v[208:211], v[44:47]\n" \
  "ds_read_b128 v[16:19], v8 offset:16384\n" \
  "v_mfma_f32_16x16x32_bf16 v[44:47], v[24:27], v[216:219], v[44:47]\n" \
  "v_mfma_f32_16x16x32_bf16 v[48:51], v[20:23], v[212:215], v[48:51]\n" \
  "ds_read_b128 v[20:23], v8 offset:17408\n" \
  "v_mfma_f32_16x16x32_bf16 v[48:51], v[24:27], v[220:223], v[48:51]\n" \
  "buffer_load_dword v80, v15, s[64:67], s84 offen sc1\n" \
  "s_add_u32 s84, s84, 0x8000\n" \
  "s_waitcnt lgkmcnt(2)\n" \
  "v_mfma_f32_16x16x32_bf16 v[48:51], v[28:31], v[228:231], v[48:51]\n" \
  "ds_read_b128 v[24:27], v8 offset:18432\n" \
  "v_mfma_f32_16x16x32_bf16 v[48:51], v[32:35], v[236:239], v[48:51]\n" \
  "v_mfma_f32_16x16x32_bf16 v[44:47], v[28:31], v[224:227], v[44:47]\n" \
  "ds_read_b128 v[28:31], v8 offset:19456\n" \
  "s_setprio 0\n" \
  "v_mfma_f32_16x16x32_bf16 v[44:47], v[32:35], v[232:235], v[44:47]\n" \
  "s_waitcnt lgkmcnt(2)\n" \
  "v_mfma_f32_16x16x32_bf16 v[44:47], v[16:19], v[240:243], v[44:47]\n" \
  "ds_read_b128 v[32:35], v8 offset:20480\n" \
  "v_mfma_f32_16x16x32_bf16 v[44:47], v[20:23], v[248:251], v[44:47]\n" \
  "buffer_load_dword v81, v15, s[64:67], s84 offen sc1\n" \
  "s_add_u32 s84, s84, 0x8000\n" \
  "v_mfma_f32_16x16x32_bf16 v[48:51], v[16:19], v[244:247], v[48:51]\n" \
  "ds_read_b128 v[16:19], v8 offset:21504\n" \
  "v_mfma_f32_16x16x32_bf16 v[48:51], v[20:23], v[252:255], v[48:51]\n" \
  "s_waitcnt lgkmcnt(2)\n" \
  "v_mfma_f32_16x16x32_bf16 v[52:55], v[24:27], v[112:115], 0\n" \
  "ds_read_b128 v[20:23], v8 offset:22528\n" \
  "v_mfma_f32_16x16x32_bf16 v[52:55], v[28:31], v[120:123], v[52:55]\n" \
  "v_mfma_f32_16x16x32_bf16 v[56:59], v[24:27], v[116:119], 0\n" \
  "ds_read_b128 v[24:27], v8 offset:23552\n" \
  "v_mfma_f32_16x16x32_bf16 v[56:59], v[28:31], v[124:127], v[56:59]\n" \
  "buffer_load_dword v82, v15, s[64:67], s84 offen sc1\n" \
  "s_add_u32 s84, s84, 0x8000\n" \
  "s_waitcnt lgkmcnt(2)\n" \
  "v_mfma_f32_16x16x32_bf16 v[56:59], v[32:35], v[132:135], v[56:59]\n" \
  "s_waitcnt vmcnt(11)\n" \
  "s_barrier\n" \
  "ds_read_b128 v[28:31], v8 offset:24576\n" \
  "s_setprio 3\n" \
  "v_mfma_f32_16x16x32_bf16 v[56:59], v[16:19], v[140:143], v[56:59]\n" \
  "v_mfma_f32_16x16x32_bf16 v[52:55], v[32:35], v[128:131], v[52:55]\n" \
  "ds_read_b128 v[32:35], v8 offset:25600\n" \
  "v_mfma_f32_16x16x32_bf16 v[52:55], v[16:19], v[136:139], v[52:55]\n" \
  "s_waitcnt lgkmcnt(2)\n" \
  "v_mfma_f32_16x16x32_bf16 v[52:55], v[20:23], v[144:147], v[52:55]\n" \
  "ds_read_b128 v[16:19], v8 offset:26624\n" \
  "v_mfma_f32_16x16x32_bf16 v[52:55], v[24:27], v[152:155], v[52:55]\n" \
  "buffer_load_dword v83, v15, s[64:67], s84 offen sc1\n" \
  "s_add_u32 s84, s84, 0x8000\n" \
  "v_mfma_f32_16x16x32_bf16 v[56:59], v[20:23], v[148:151], v[56:59]\n" \
  "ds_read_b128 v[20:23], v8 offset:27648\n" \
  "v_mfma_f32_16x16x32_bf16 v[56:59], v[24:27], v[156:159], v[56:59]\n" \
  "s_waitcnt lgkmcnt(2)\n" \
  "v_mfma_f32_16x16x32_bf16 v[56:59], v[28:31], v[164:167], v[56:59]\n" \
  "ds_read_b128 v[24:27], v8 offset:28672\n" \
  "v_mfma_f32_16x16x32_bf16 v[56:59], v[32:35], v[172:175], v[56:59]\n" \
  "v_mfma_f32_16x16x32_bf16 v[52:55], v[28:31], v[160:163], v[52:55]\n" \
  "ds_read_b128 v[28:31], v8 offset:29696\n" \
  "v_mfma_f32_16x16x32_bf16 v[52:55], v[32:35], v[168:171], v[52:55]\n" \
  "buffer_load_dword v84, v15, s[64:67], s84 offen sc1\n" \
  "s_add_u32 s84, s84, 0x8000\n" \
  "s_waitcnt lgkmcnt(2)\n" \
  "v_mfma_f32_16x16x32_bf16 v[52:55], v[16:19], v[176:179], v[52:55]\n" \
  "ds_read_b128 v[32:35], v8 offset:30720\n" \
  "v_mfma_f32_16x16x32_bf16 v[52:55], v[20:23], v[184:187], v[52:55]\n" \
  "v_mfma_f32_16x16x32_bf16 v[56:59], v[16:19], v[180:183], v[56:59]\n" \
  "ds_read_b128 v[16:19], v8 offset:31744\n" \
  "v_mfma_f32_16x16x32_bf16 v[56:59], v[20:23], v[188:191], v[56:59]\n" \
  "s_waitcnt lgkmcnt(2)\n" \
  "v_mfma_f32_16x16x32_bf16 v[56:59], v[24:27], v[196:199], v[56:59]\n" \
  "ds_read_b128 v[20:23], v8 offset:32768\n" \
  "v_mfma_f32_16x16x32_bf16 v[56:59], v[28:31], v[204:207], v[56:59]\n" \
  "v_and_b32 v7, 0x1fff, v15\n" \
  "buffer_load_dword v0, v7, s[64:67], s84 offen sc1\n" \
  "v_mfma_f32_16x16x32_bf16 v[52:55], v[24:27], v[192:195], v[52:55]\n" \
  "ds_read_b128 v[24:27], v8 offset:33792\n" \
  "v_mfma_f32_16x16x32_bf16 v[52:55], v[28:31], v[200:203], v[52:55]\n" \
  "s_waitcnt lgkmcnt(2)\n" \
  "v_mfma_f32_16x16x32_bf16 v[52:55], v[32:35], v[208:211], v[52:55]\n" \
  "ds_read_b128 v[28:31], v8 offset:34816\n" \
  "v_mfma_f32_16x16x32_bf16 v[52:55], v[16:19], v[216:219], v[52:55]\n" \
  "v_mfma_f32_16x16x32_bf16 v[56:59], v[32:35], v[212:215], v[56:59]\n" \
  "ds_read_b128 v[32:35], v8 offset:35840\n" \
  "v_mfma_f32_16x16x32_bf16 v[56:59], v[16:19], v[220:223], v[56:59]\n" \
  "s_add_u32 m0, s69, 114688\n" \
  "s_add_u32 s68, s68, 0x2000\n" \
  "buffer_load_dwordx4 v8, s[64:67], s68 offen sc1 lds\n" \
  "s_waitcnt lgkmcnt(2)\n" \
  "v_mfma_f32_16x16x32_bf16 v[56:59], v[20:23], v[228:231], v[56:59]\n" \
  "ds_read_b128 v[16:19], v8 offset:36864\n" \
  "v_mfma_f32_16x16x32_bf16 v[56:59], v[24:27], v[236:239], v[56:59]\n" \
  "v_mfma_f32_16x16x32_bf16 v[52:55], v[20:23], v[224:227], v[52:55]\n" \
  "ds_read_b128 v[20:23], v8 offset:37888\n" \
  "v_mfma_f32_16x16x32_bf16 v[52:55], v[24:27], v[232:235], v[52:55]\n" \
  "s_waitcnt lgkmcnt(2)\n" \
  "v_mfma_f32_16x16x32_bf16 v[52:55], v[28:31], v[240:243], v[52:55]\n" \
  "ds_read_b128 v[24:27], v8 offset:38912\n" \
  "v_mfma_f32_16x16x32_bf16 v[52:55], v[32:35], v[248:251], v[52:55]\n" \
  "s_add_u32 m0, s69, 122880\n" \
  "s_add_u32 s68, s68, 0x2000\n" \
  "buffer_load_dwordx4 v8, s[64:67], s68 offen sc1 lds\n" \
  "v_mfma_f32_16x16x32_bf16 v[56:59], v[28:31], v[244:247], v[56:59]\n" \
  "ds_read_b128 v[28:31], v8 offset:39936\n" \
  "v_mfma_f32_16x16x32_bf16 v[56:59], v[32:35], v[252:255], v[56:59]\n" \
  "s_waitcnt lgkmcnt(2)\n" \
  "v_mfma_f32_16x16x32_bf16 v[92:95], v[16:19], v[112:115], 0\n" \
  "ds_read_b128 v[32:35], v8 offset:40960\n" \
  "v_mfma_f32_16x16x32_bf16 v[92:95], v[20:23], v[120:123], v[92:95]\n" \
  "v_mfma_f32_16x16x32_bf16 v[96:99], v[16:19], v[116:119], 0\n" \
  "ds_read_b128 v[16:19], v8 offset:41984\n" \
  "v_mfma_f32_16x16x32_bf16 v[96:99], v[20:23], v[124:127], v[96:99]\n" \
  "s_add_u32 m0, s69, 131072\n" \
  "s_add_u32 s68, s68, 0x2000\n" \
  "buffer_load_dwordx4 v8, s[64:67], s68 offen sc1 lds\n" \
  "s_waitcnt lgkmcnt(2)\n" \
  "v_mfma_f32_16x16x32_bf16 v[96:99], v[24:27], v[132:135], v[96:99]\n" \
  "ds_read_b128 v[20:23], v8 offset:43008\n" \
  "v_mfma_f32_16x16x32_bf16 v[96:99], v[28:31], v[140:143], v[96:99]\n" \
  "v_mfma_f32_16x16x32_bf16 v[92:95], v[24:27], v[128:131], v[92:95]\n" \
  "ds_read_b128 v[24:27], v8 offset:44032\n" \
  "v_mul_f32 v44, v44, v52\n" \
  "v_mfma_f32_16x16x32_bf16 v[92:95], v[28:31], v[136:139], v[92:95]\n" \
  "v_mul_f32 v45, v45, v53\n" \
  "s_waitcnt lgkmcnt(2)\n" \
  "v_mfma_f32_16x16x32_bf16 v[92:95], v[32:35], v[144:147], v[92:95]\n" \
  "ds_read_b128 v[28:31], v8 offset:45056\n" \
  "v_mul_f32 v46, v46, v54\n" \
  "v_mfma_f32_16x16x32_bf16 v[92:95], v[16:19], v[152:155], v[92:95]\n" \
  "v_mul_f32 v47, v47, v55\n" \
  "s_add_u32 m0, s69, 139264\n" \
  "s_add_u32 s68, s68, 0x2000\n" \
  "buffer_load_dwordx4 v8, s[64:67], s68 offen sc1 lds\n" \
  "v_mfma_f32_16x16x32_bf16 v[96:99], v[32:35], v[148:151], v[96:99]\n" \
  "ds_read_b128 v[32:35], v8 offset:46080\n" \
  "v_cvt_pk_bf16_f32 v38, v44, v45\n" \
  "v_mfma_f32_16x16x32_bf16 v[96:99], v[16:19], v[156:159], v[96:99]\n" \
  "v_cvt_pk_bf16_f32 v39, v46, v47\n" \
  "s_waitcnt lgkmcnt(2)\n" \
  "v_mfma_f32_16x16x32_bf16 v[96:99], v[20:23], v[164:167], v[96:99]\n" \
  "ds_read_b128 v[16:19], v8 offset:47104\n" \
  "v_mul_f32 v48, v48, v56\n" \
  "v_mfma_f32_16x16x32_bf16 v[96:99], v[24:27], v[172:175], v[96:99]\n" \
  "v_mul_f32 v49, v49, v57\n" \
  "v_mfma_f32_16x16x32_bf16 v[92:95], v[20:23], v[160:163], v[92:95]\n" \
  "ds_read_b128 v[20:23], v8 offset:48128\n" \
  "v_mul_f32 v50, v50, v58\n" \
  "v_mfma_f32_16x16x32_bf16 v[92:95], v[24:27], v[168:171], v[92:95]\n" \
  "v_mul_f32 v51, v51, v59\n" \
  "s_add_u32 m0, s69, 0\n" \
  "s_add_u32 s68, s68, 0x2000\n" \
  "buffer_load_dwordx4 v8, s[64:67], s68 offen sc1 lds\n" \
  "s_waitcnt lgkmcnt(2)\n" \
  "v_mfma_f32_16x16x32_bf16 v[92:95], v[28:31], v[176:179], v[92:95]\n" \
  "ds_read_b128 v[24:27], v4 offset:0\n" \
  "v_cvt_pk_bf16_f32 v42, v48, v49\n" \
  "v_mfma_f32_16x16x32_bf16 v[92:95], v[32:35], v[184:187], v[92:95]\n" \
  "v_cvt_pk_bf16_f32 v43, v50, v51\n" \
  "v_mfma_f32_16x16x32_bf16 v[96:99], v[28:31], v[180:183], v[96:99]\n" \
  "ds_read_b128 v[28:31], v4 offset:1024\n" \
  "buffer_store_dwordx4 v[36:39], v8, s[64:67], s74 offen offset:2048 sc1\n" \
  "v_mfma_f32_16x16x32_bf16 v[96:99], v[32:35], v[188:191], v[96:99]\n" \
  "s_waitcnt lgkmcnt(2)\n" \
  "v_mfma_f32_16x16x32_bf16 v[96:99], v[16:19], v[196:199], v[96:99]\n" \
  "ds_read_b128 v[32:35], v4 offset:2048\n" \
  "v_mfma_f32_16x16x32_bf16 v[96:99], v[20:23], v[204:207], v[96:99]\n" \
  "s_add_u32 m0, s69, 8192\n" \
  "s_add_u32 s68, s68, 0x2000\n" \
  "buffer_load_dwordx4 v8, s[64:67], s68 offen sc1 lds\n" \
  "v_mfma_f32_16x16x32_bf16 v[92:95], v[16:19], v[192:195], v[92:95]\n" \
  "ds_read_b128 v[16:19], v4 offset:3072\n" \
  "v_mfma_f32_16x16x32_bf16 v[92:95], v[20:23], v[200:203], v[92:95]\n" \
  "s_waitcnt lgkmcnt(2)\n" \
  "v_mfma_f32_16x16x32_bf16 v[92:95], v[24:27], v[208:211], v[92:95]\n" \
  "ds_read_b128 v[20:23], v4 offset:4096\n" \
  "v_mfma_f32_16x16x32_bf16 v[92:95], v[28:31], v[216:219], v[92:95]\n" \
  "v_mfma_f32_16x16x32_bf16 v[96:99], v[24:27], v[212:215], v[96:99]\n" \
  "ds_read_b128 v[24:27], v4 offset:5120\n" \
  "v_mfma_f32_16x16x32_bf16 v[96:99], v[28:31], v[220:223], v[96:99]\n" \
  "s_waitcnt lgkmcnt(2)\n" \
  "v_mfma_f32_16x16x32_bf16 v[96:99], v[32:35], v[228:231], v[96:99]\n" \
  "ds_read_b128 v[28:31], v4 offset:6144\n" \
  "s_setprio 2\n" \
  "v_mfma_f32_16x16x32_bf16 v[96:99], v[16:19], v[236:239], v[96:99]\n" \
  "v_mfma_f32_16x16x32_bf16 v[92:95], v[32:35], v[224:227], v[92:95]\n" \
  "ds_read_b128 v[32:35], v4 offset:7168\n" \
  "v_mfma_f32_16x16x32_bf16 v[92:95], v[16:19], v[232:235], v[92:95]\n" \
  "s_waitcnt lgkmcnt(2)\n" \
  "v_mfma_f32_16x16x32_bf16 v[92:95], v[20:23], v[240:243], v[92:95]\n" \
  "ds_read_b128 v[16:19], v4 offset:8192\n" \
  "v_mfma_f32_16x16x32_bf16 v[92:95], v[24:27], v[248:251], v[92:95]\n" \
  "v_mfma_f32_16x16x32_bf16 v[96:99], v[20:23], v[244:247], v[96:99]\n" \
  "ds_read_b128 v[20:23], v4 offset:9216\n" \
  "v_mfma_f32_16x16x32_bf16 v[96:99], v[24:27], v[252:255], v[96:99]\n" \
  "s_waitcnt lgkmcnt(2)\n" \
  "v_mfma_f32_16x16x32_bf16 v[100:103], v[28:31], v[112:115], 0\n" \
  "ds_read_b128 v[24:27], v4 offset:10240\n" \
  "v_mfma_f32_16x16x32_bf16 v[100:103], v[32:35], v[120:123], v[100:103]\n" \
  "v_mfma_f32_16x16x32_bf16 v[104:107], v[28:31], v[116:119], 0\n" \
  "ds_read_b128 v[28:31], v4 offset:11264\n" \
  "v_mfma_f32_16x16x32_bf16 v[104:107], v[32:35], v[124:127], v[104:107]\n" \
  "s_waitcnt lgkmcnt(2)\n" \
  "v_mfma_f32_16x16x32_bf16 v[104:107], v[16:19], v[132:135], v[104:107]\n" \
  "ds_read_b128 v[32:35], v4 offset:12288\n" \
  "s_waitcnt vmcnt(7)\n" \
  "v_mfma_f32_16x16x32_bf16 v[104:107], v[20:23], v[140:143], v[104:107]\n" \
  "v_add_f32 v1, v76, v77\n" \
  "v_mfma_f32_16x16x32_bf16 v[100:103], v[16:19], v[128:131], v[100:103]\n" \
  "ds_read_b128 v[16:19], v4 offset:13312\n" \
  "v_add_f32 v1, v1, v78\n" \
  "v_mfma_f32_16x16x32_bf16 v[100:103], v[20:23], v[136:139], v[100:103]\n" \
  "v_add_f32 v1, v1, v79\n" \
  "s_waitcnt lgkmcnt(2)\n" \
  "v_mfma_f32_16x16x32_bf16 v[100:103], v[24:27], v[144:147], v[100:103]\n" \
  "ds_read_b128 v[20:23], v4 offset:14336\n" \
  "v_add_f32 v1, v1, v80\n" \
  "v_mfma_f32_16x16x32_bf16 v[100:103], v[28:31], v[152:155], v[100:103]\n" \
  "v_add_f32 v1, v1, v81\n" \
  "v_mfma_f32_16x16x32_bf16 v[104:107], v[24:27], v[148:151], v[104:107]\n" \
  "ds_read_b128 v[24:27], v4 offset:15360\n" \
  "v_add_f32 v1, v1, v82\n" \
  "v_mfma_f32_16x16x32_bf16 v[104:107], v[28:31], v[156:159], v[104:107]\n" \
  "v_add_f32 v1, v1, v83\n" \
  "s_waitcnt lgkmcnt(2)\n" \
  "v_mfma_f32_16x16x32_bf16 v[104:107], v[32:35], v[164:167], v[104:107]\n" \
  "ds_read_b128 v[28:31], v4 offset:16384\n" \
  "v_add_f32 v1, v1, v84\n" \
  "v_mfma_f32_16x16x32_bf16 v[104:107], v[16:19], v[172:175], v[104:107]\n" \
  "buffer_store_dwordx4 v[40:43], v8, s[64:67], s74 offen offset:3072 sc1\n" \
  "v_mfma_f32_16x16x32_bf16 v[100:103], v[32:35], v[160:163], v[100:103]\n" \
  "ds_read_b128 v[32:35], v4 offset:17408\n" \
  "v_mfma_f32_16x16x32_bf16 v[100:103], v[16:19], v[168:171], v[100:103]\n" \
  "s_waitcnt lgkmcnt(2)\n" \
  "v_mfma_f32_16x16x32_bf16 v[100:103], v[20:23], v[176:179], v[100:103]\n" \
  "ds_read_b128 v[16:19], v4 offset:18432\n" \
  "v_mfma_f32_16x16x32_bf16 v[100:103], v[24:27], v[184:187], v[100:103]\n" \
  "v_mfma_f32_16x16x32_bf16 v[104:107], v[20:23], v[180:183], v[104:107]\n" \
  "ds_read_b128 v[20:23], v4 offset:19456\n" \
  "v_mfma_f32_16x16x32_bf16 v[104:107], v[24:27], v[188:191], v[104:107]\n" \
  "s_waitcnt lgkmcnt(2)\n" \
  "v_mfma_f32_16x16x32_bf16 v[104:107], v[28:31], v[196:199], v[104:107]\n" \
  "ds_read_b128 v[24:27], v4 offset:20480\n" \
  "v_mfma_f32_16x16x32_bf16 v[104:107], v[32:35], v[204:207], v[104:107]\n" \
  "v_mfma_f32_16x16x32_bf16 v[100:103], v[28:31], v[192:195], v[100:103]\n" \
  "ds_read_b128 v[28:31], v4 offset:21504\n" \
  "v_mfma_f32_16x16x32_bf16 v[100:103], v[32:35], v[200:203], v[100:103]\n" \
  "s_waitcnt lgkmcnt(2)\n" \
  "v_mfma_f32_16x16x32_bf16 v[100:103], v[16:19], v[208:211], v[100:103]\n" \
  "ds_read_b128 v[32:35], v4 offset:22528\n" \
  "v_mfma_f32_16x16x32_bf16 v[100:103], v[20:23], v[216:219], v[100:103]\n" \
  "v_mfma_f32_16x16x32_bf16 v[104:107], v[16:19], v[212:215], v[104:107]\n" \
  "ds_read_b128 v[16:19], v4 offset:23552\n" \
  "v_mfma_f32_16x16x32_bf16 v[104:107], v[20:23], v[220:223], v[104:107]\n" \
  "s_waitcnt lgkmcnt(2)\n" \
  "v_mfma_f32_16x16x32_bf16 v[104:107], v[24:27], v[228:231], v[104:107]\n" \
  "ds_read_b128 v[20:23], v4 offset:24576\n" \
  "v_mfma_f32_16x16x32_bf16 v[104:107], v[28:31], v[236:239], v[104:107]\n" \
  "v_mfma_f32_16x16x32_bf16 v[100:103], v[24:27], v[224:227], v[100:103]\n" \
  "ds_read_b128 v[24:27], v4 offset:25600\n" \
  "v_mfma_f32_16x16x32_bf16 v[100:103], v[28:31], v[232:235], v[100:103]\n" \
  "s_waitcnt lgkmcnt(2)\n" \
  "v_mfma_f32_16x16x32_bf16 v[100:103], v[32:35], v[240:243], v[100:103]\n" \
  "ds_read_b128 v[28:31], v4 offset:26624\n" \
  "v_mfma_f32_16x16x32_bf16 v[100:103], v[16:19], v[248:251], v[100:103]\n" \
  "v_mfma_f32_16x16x32_bf16 v[104:107], v[32:35], v[244:247], v[104:107]\n" \
  "ds_read_b128 v[32:35], v4 offset:27648\n" \
  "s_setprio 1\n" \
  "v_mfma_f32_16x16x32_bf16 v[104:107], v[16:19], v[252:255], v[104:107]\n" \
  "s_waitcnt lgkmcnt(2)\n" \
  "v_mfma_f32_16x16x32_bf16 v[60:63], v[20:23], v[112:115], 0\n" \
  "ds_read_b128 v[16:19], v4 offset:28672\n" \
  "v_mfma_f32_16x16x32_bf16 v[60:63], v[24:27], v[120:123], v[60:63]\n" \
  "v_mfma_f32_16x16x32_bf16 v[64:67], v[20:23], v[116:119], 0\n" \
  "ds_read_b128 v[20:23], v4 offset:29696\n" \
  "v_mfma_f32_16x16x32_bf16 v[64:67], v[24:27], v[124:127], v[64:67]\n" \
  "s_waitcnt lgkmcnt(2)\n" \
  "v_mfma_f32_16x16x32_bf16 v[64:67], v[28:31], v[132:135], v[64:67]\n" \
  "ds_read_b128 v[24:27], v4 offset:30720\n" \
  "v_mfma_f32_16x16x32_bf16 v[64:67], v[32:35], v[140:143], v[64:67]\n" \
  "v_mfma_f32_16x16x32_bf16 v[60:63], v[28:31], v[128:131], v[60:63]\n" \
  "ds_read_b128 v[28:31], v4 offset:31744\n" \
  "v_mul_f32 v92, v92, v100\n" \
  "v_mfma_f32_16x16x32_bf16 v[60:63], v[32:35], v[136:139], v[60:63]\n" \
  "v_mul_f32 v93, v93, v101\n" \
  "s_waitcnt lgkmcnt(2)\n" \
  "v_mfma_f32_16x16x32_bf16 v[60:63], v[16:19], v[144:147], v[60:63]\n" \
  "ds_read_b128 v[32:35], v4 offset:32768\n" \
  "v_mul_f32 v94, v94, v102\n" \
  "v_mfma_f32_16x16x32_bf16 v[60:63], v[20:23], v[152:155], v[60:63]\n" \
  "v_mul_f32 v95, v95, v103\n" \
  "v_mfma_f32_16x16x32_bf16 v[64:67], v[16:19], v[148:151], v[64:67]\n" \
  "ds_read_b128 v[16:19], v4 offset:33792\n" \
  "v_cvt_pk_bf16_f32 v36, v92, v93\n" \
  "v_mfma_f32_16x16x32_bf16 v[64:67], v[20:23], v[156:159], v[64:67]\n" \
  "v_cvt_pk_bf16_f32 v37, v94, v95\n" \
  "s_waitcnt lgkmcnt(2)\n" \
  "v_mfma_f32_16x16x32_bf16 v[64:67], v[24:27], v[164:167], v[64:67]\n" \
  "ds_read_b128 v[20:23], v4 offset:34816\n" \
  "v_mul_f32 v96, v96, v104\n" \
  "v_mfma_f32_16x16x32_bf16 v[64:67], v[28:31], v[172:175], v[64:67]\n" \
  "v_mul_f32 v97, v97, v105\n" \
  "v_mfma_f32_16x16x32_bf16 v[60:63], v[24:27], v[160:163], v[60:63]\n" \
  "ds_read_b128 v[24:27], v4 offset:35840\n" \
  "v_mul_f32 v98, v98, v106\n" \
  "v_mfma_f32_16x16x32_bf16 v[60:63], v[28:31], v[168:171], v[60:63]\n" \
  "v_mul_f32 v99, v99, v107\n" \
  "s_waitcnt lgkmcnt(2)\n" \
  "v_mfma_f32_16x16x32_bf16 v[60:63], v[32:35], v[176:179], v[60:63]\n" \
  "ds_read_b128 v[28:31], v4 offset:36864\n" \
  "v_cvt_pk_bf16_f32 v40, v96, v97\n" \
  "v_mfma_f32_16x16x32_bf16 v[60:63], v[16:19], v[184:187], v[60:63]\n" \
  "v_cvt_pk_bf16_f32 v41, v98, v99\n" \
  "v_mfma_f32_16x16x32_bf16 v[64:67], v[32:35], v[180:183], v[64:67]\n" \
  "ds_read_b128 v[32:35], v4 offset:37888\n" \
  "s_setprio 0\n" \
  "v_mfma_f32_16x16x32_bf16 v[64:67], v[16:19], v[188:191], v[64:67]\n" \
  "s_waitcnt lgkmcnt(2)\n" \
  "v_mfma_f32_16x16x32_bf16 v[64:67], v[20:23], v[196:199], v[64:67]\n" \
  "ds_read_b128 v[16:19], v4 offset:38912\n" \
  "v_mfma_f32_16x16x32_bf16 v[64:67], v[24:27], v[204:207], v[64:67]\n" \
  "v_mfma_f32_16x16x32_bf16 v[60:63], v[20:23], v[192:195], v[60:63]\n" \
  "ds_read_b128 v[20:23], v4 offset:39936\n" \
  "v_mfma_f32_16x16x32_bf16 v[60:63], v[24:27], v[200:203], v[60:63]\n" \
  "s_waitcnt lgkmcnt(2)\n" \
  "v_mfma_f32_16x16x32_bf16 v[60:63], v[28:31], v[208:211], v[60:63]\n" \
  "ds_read_b128 v[24:27], v4 offset:40960\n" \
  "v_mfma_f32_16x16x32_bf16 v[60:63], v[32:35], v[216:219], v[60:63]\n" \
  "v_mfma_f32_16x16x32_bf16 v[64:67], v[28:31], v[212:215], v[64:67]\n" \
  "ds_read_b128 v[28:31], v4 offset:41984\n" \
  "v_mfma_f32_16x16x32_bf16 v[64:67], v[32:35], v[220:223], v[64:67]\n" \
  "s_waitcnt lgkmcnt(2)\n" \
  "v_mfma_f32_16x16x32_bf16 v[64:67], v[16:19], v[228:231], v[64:67]\n" \
  "ds_read_b128 v[32:35], v4 offset:43008\n" \
  "s_waitcnt vmcnt(0)\n" \
  "v_mov_b32 v7, 1\n" \
  "s_mov_b64 exec, 1\n" \
  "buffer_store_dword v7, v8, s[64:67], s77 offen sc1\n" \
  "s_mov_b64 exec, -1\n" \
  "v_mfma_f32_16x16x32_bf16 v[64:67], v[20:23], v[236:239], v[64:67]\n" \
  "v_mfma_f32_16x16x32_bf16 v[60:63], v[16:19], v[224:227], v[60:63]\n" \
  "ds_read_b128 v[16:19], v4 offset:44032\n" \
  "v_mfma_f32_16x16x32_bf16 v[60:63], v[20:23], v[232:235], v[60:63]\n" \
  "s_waitcnt lgkmcnt(2)\n" \
  "v_mfma_f32_16x16x32_bf16 v[60:63], v[24:27], v[240:243], v[60:63]\n" \
  "s_barrier\n" \
  "ds_read_b128 v[20:23], v4 offset:45056\n" \
  "s_setprio 3\n" \
  "v_mfma_f32_16x16x32_bf16 v[60:63], v[28:31], v[248:251], v[60:63]\n" \
  "s_add_u32 m0, s69, 16384\n" \
  "s_add_u32 s68, s68, 0x2000\n" \
  "buffer_load_dwordx4 v8, s[64:67], s68 offen sc1 lds\n" \
  "v_mfma_f32_16x16x32_bf16 v[64:67], v[24:27], v[244:247], v[64:67]\n" \
  "ds_read_b128 v[24:27], v4 offset:46080\n" \
  "v_mfma_f32_16x16x32_bf16 v[64:67], v[28:31], v[252:255], v[64:67]\n" \
  "s_waitcnt lgkmcnt(2)\n" \
  "v_mfma_f32_16x16x32_bf16 v[68:71], v[32:35], v[112:115], 0\n" \
  "ds_read_b128 v[28:31], v4 offset:47104\n" \
  "v_mfma_f32_16x16x32_bf16 v[68:71], v[16:19], v[120:123], v[68:71]\n" \
  "s_add_u32 m0, s69, 24576\n" \
  "s_add_u32 s68, s68, 0x2000\n" \
  "buffer_load_dwordx4 v8, s[64:67], s68 offen sc1 lds\n" \
  "v_mfma_f32_16x16x32_bf16 v[72:75], v[32:35], v[116:119], 0\n" \
  "ds_read_b128 v[32:35], v4 offset:48128\n" \
  "v_mfma_f32_16x16x32_bf16 v[72:75], v[16:19], v[124:127], v[72:75]\n" \
  "s_waitcnt lgkmcnt(2)\n" \
  "v_mfma_f32_16x16x32_bf16 v[72:75], v[20:23], v[132:135], v[72:75]\n" \
  "ds_read_b128 v[16:19], v5 offset:0\n" \
  "v_mfma_f32_16x16x32_bf16 v[72:75], v[24:27], v[140:143], v[72:75]\n" \
  "v_mfma_f32_16x16x32_bf16 v[68:71], v[20:23], v[128:131], v[68:71]\n" \
  "ds_read_b128 v[20:23], v5 offset:1024\n" \
  "v_mfma_f32_16x16x32_bf16 v[68:71], v[24:27], v[136:139], v[68:71]\n" \
  "s_add_u32 m0, s69, 32768\n" \
  "s_add_u32 s68, s68, s70\n" \
  "buffer_load_dwordx4 v8, s[64:67], s68 offen sc1 lds\n" \
  "s_waitcnt lgkmcnt(2)\n" \
  "v_mfma_f32_16x16x32_bf16 v[68:71], v[28:31], v[144:147], v[68:71]\n" \
  "ds_read_b128 v[24:27], v5 offset:2048\n" \
  "v_mfma_f32_16x16x32_bf16 v[68:71], v[32:35], v[152:155], v[68:71]\n" \
  "v_mfma_f32_16x16x32_bf16 v[72:75], v[28:31], v[148:151], v[72:75]\n" \
  "ds_read_b128 v[28:31], v5 offset:3072\n" \
  "v_mfma_f32_16x16x32_bf16 v[72:75], v[32:35], v[156:159], v[72:75]\n" \
  "s_waitcnt lgkmcnt(2)\n" \
  "v_mfma_f32_16x16x32_bf16 v[72:75], v[16:19], v[164:167], v[72:75]\n" \
  "ds_read_b128 v[32:35], v5 offset:4096\n" \
  "v_mfma_f32_16x16x32_bf16 v[72:75], v[20:23], v[172:175], v[72:75]\n" \
  "s_add_u32 m0, s69, 40960\n" \
  "s_add_u32 s68, s68, s71\n" \
  "buffer_load_dwordx4 v8, s[64:67], s68 offen sc1 lds\n" \
  "v_mfma_f32_16x16x32_bf16 v[68:71], v[16:19], v[160:163], v[68:71]\n" \
  "ds_read_b128 v[16:19], v5 offset:5120\n" \
  "v_mfma_f32_16x16x32_bf16 v[68:71], v[20:23], v[168:171], v[68:71]\n" \
  "s_waitcnt lgkmcnt(2)\n" \
  "v_mfma_f32_16x16x32_bf16 v[68:71], v[24:27], v[176:179], v[68:71]\n" \
  "ds_read_b128 v[20:23], v5 offset:6144\n" \
  "v_mfma_f32_16x16x32_bf16 v[68:71], v[28:31], v[184:187], v[68:71]\n" \
  "v_mfma_f32_16x16x32_bf16 v[72:75], v[24:27], v[180:183], v[72:75]\n" \
  "ds_read_b128 v[24:27], v5 offset:7168\n" \
  "v_mfma_f32_16x16x32_bf16 v[72:75], v[28:31], v[188:191], v[72:75]\n" \
  "s_add_u32 m0, s69, 49152\n" \
  "s_add_u32 s68, s68, 0x2000\n" \
  "buffer_load_dwordx4 v8, s[64:67], s68 offen sc1 lds\n" \
  "s_waitcnt lgkmcnt(2)\n" \
  "v_mfma_f32_16x16x32_bf16 v[72:75], v[32:35], v[196:199], v[72:75]\n" \
  "ds_read_b128 v[28:31], v5 offset:8192\n" \
  "v_mfma_f32_16x16x32_bf16 v[72:75], v[16:19], v[204:207], v[72:75]\n" \
  "v_mfma_f32_16x16x32_bf16 v[68:71], v[32:35], v[192:195], v[68:71]\n" \
  "ds_read_b128 v[32:35], v5 offset:9216\n" \
  "v_mfma_f32_16x16x32_bf16 v[68:71], v[16:19], v[200:203], v[68:71]\n" \
  "s_waitcnt lgkmcnt(2)\n" \
  "v_mfma_f32_16x16x32_bf16 v[68:71], v[20:23], v[208:211], v[68:71]\n" \
  "ds_read_b128 v[16:19], v5 offset:10240\n" \
  "v_mfma_f32_16x16x32_bf16 v[68:71], v[24:27], v[216:219], v[68:71]\n" \
  "s_add_u32 m0, s69, 57344\n" \
  "s_add_u32 s68, s68, 0x2000\n" \
  "buffer_load_dwordx4 v8, s[64:67], s68 offen sc1 lds\n" \
  "v_mfma_f32_16x16x32_bf16 v[72:75], v[20:23], v[212:215], v[72:75]\n" \
  "ds_read_b128 v[20:23], v5 offset:11264\n" \
  "v_mfma_f32_16x16x32_bf16 v[72:75], v[24:27], v[220:223], v[72:75]\n" \
  "s_waitcnt lgkmcnt(2)\n" \
  "v_mfma_f32_16x16x32_bf16 v[72:75], v[28:31], v[228:231], v[72:75]\n" \
  "ds_read_b128 v[24:27], v5 offset:12288\n" \
  "s_setprio 2\n" \
  "buffer_load_dword v6, v12, s[64:67], 0 offen sc1\n" \
  "v_mfma_f32_16x16x32_bf16 v[72:75], v[32:35], v[236:239], v[72:75]\n" \
  "v_mfma_f32_16x16x32_bf16 v[68:71], v[28:31], v[224:227], v[68:71]\n" \
  "ds_read_b128 v[28:31], v5 offset:16384\n" \
  "v_mfma_f32_16x16x32_bf16 v[68:71], v[32:35], v[232:235], v[68:71]\n" \
  "s_add_u32 m0, s69, 65536\n" \
  "s_add_u32 s68, s68, 0x2000\n" \
  "buffer_load_dwordx4 v8, s[64:67], s68 offen sc1 lds\n" \
  "s_waitcnt lgkmcnt(2)\n" \
  "v_mfma_f32_16x16x32_bf16 v[68:71], v[16:19], v[240:243], v[68:71]\n" \
  "ds_read_b128 v[32:35], v5 offset:14336\n" \
  "v_mfma_f32_16x16x32_bf16 v[68:71], v[20:23], v[248:251], v[68:71]\n" \
  "v_mfma_f32_16x16x32_bf16 v[72:75], v[16:19], v[244:247], v[72:75]\n" \
  "ds_read_b128 v[16:19], v5 offset:18432\n" \
  "v_mfma_f32_16x16x32_bf16 v[72:75], v[20:23], v[252:255], v[72:75]\n" \
  "s_waitcnt lgkmcnt(2)\n" \
  "v_mfma_f32_16x16x32_bf16 v[44:47], v[24:27], v[112:115], 0\n" \
  "ds_read_b128 v[20:23], v5 offset:13312\n" \
  "v_mfma_f32_16x16x32_bf16 v[44:47], v[28:31], v[120:123], v[44:47]\n" \
  "s_add_u32 m0, s69, 73728\n" \
  "s_add_u32 s68, s68, 0x2000\n" \
  "buffer_load_dwordx4 v8, s[64:67], s68 offen sc1 lds\n" \
  "v_mfma_f32_16x16x32_bf16 v[48:51], v[24:27], v[116:119], 0\n" \
  "ds_read_b128 v[24:27], v5 offset:17408\n" \
  "v_mfma_f32_16x16x32_bf16 v[48:51], v[28:31], v[124:127], v[48:51]\n" \
  "s_waitcnt lgkmcnt(2)\n" \
  "v_mfma_f32_16x16x32_bf16 v[52:55], v[32:35], v[112:115], 0\n" \
  "ds_read_b128 v[28:31], v5 offset:15360\n" \
  "v_mfma_f32_16x16x32_bf16 v[52:55], v[16:19], v[120:123], v[52:55]\n" \
  "v_mfma_f32_16x16x32_bf16 v[56:59], v[32:35], v[116:119], 0\n" \
  "ds_read_b128 v[32:35], v5 offset:19456\n" \
  "v_mul_f32 v60, v60, v68\n" \
  "v_mfma_f32_16x16x32_bf16 v[56:59], v[16:19], v[124:127], v[56:59]\n" \
  "v_mul_f32 v61, v61, v69\n" \
  "s_add_u32 m0, s69, 81920\n" \
  "s_add_u32 s68, s68, 0x2000\n" \
  "buffer_load_dwordx4 v8, s[64:67], s68 offen sc1 lds\n" \
  "s_waitcnt lgkmcnt(2)\n" \
  "v_mfma_f32_16x16x32_bf16 v[92:95], v[20:23], v[112:115], 0\n" \
  "ds_read_b128 v[16:19], v5 offset:20480\n" \
  "v_mul_f32 v62, v62, v70\n" \
  "v_mfma_f32_16x16x32_bf16 v[92:95], v[24:27], v[120:123], v[92:95]\n" \
  "v_mul_f32 v63, v63, v71\n" \
  "v_mfma_f32_16x16x32_bf16 v[96:99], v[20:23], v[116:119], 0\n" \
  "ds_read_b128 v[20:23], v5 offset:24576\n" \
  "v_cvt_pk_bf16_f32 v38, v60, v61\n" \
  "v_mfma_f32_16x16x32_bf16 v[96:99], v[24:27], v[124:127], v[96:99]\n" \
  "v_cvt_pk_bf16_f32 v39, v62, v63\n" \
  "s_waitcnt lgkmcnt(2)\n" \
  "v_mfma_f32_16x16x32_bf16 v[100:103], v[28:31], v[112:115], 0\n" \
  "ds_read_b128 v[24:27], v5 offset:22528\n" \
  "v_mul_f32 v64, v64, v72\n" \
  "v_mfma_f32_16x16x32_bf16 v[100:103], v[32:35], v[120:123], v[100:103]\n" \
  "v_mul_f32 v65, v65, v73\n" \
  "v_mfma_f32_16x16x32_bf16 v[104:107], v[28:31], v[116:119], 0\n" \
  "ds_read_b128 v[28:31], v5 offset:26624\n" \
  "s_setprio 1\n" \
  "v_mul_f32 v66, v66, v74\n" \
  "v_mfma_f32_16x16x32_bf16 v[104:107], v[32:35], v[124:127], v[104:107]\n" \
  "v_mul_f32 v67, v67, v75\n" \
  "s_waitcnt lgkmcnt(2)\n" \
  "v_mfma_f32_16x16x32_bf16 v[44:47], v[16:19], v[128:131], v[44:47]\n" \
  "ds_read_b128 v[32:35], v5 offset:21504\n" \
  "v_cvt_pk_bf16_f32 v42, v64, v65\n" \
  "s_waitcnt vmcnt(3)\n" \
  "v_cmp_gt_u32 vcc, 1, v6\n" \
  "s_cbranch_vccnz 41f\n" \
  "31:\n" \
  "v_mfma_f32_16x16x32_bf16 v[44:47], v[20:23], v[136:139], v[44:47]\n" \
  "v_cvt_pk_bf16_f32 v43, v66, v67\n" \
  "s_add_u32 s76, s73, 0\n" \
  "buffer_load_dwordx4 v[112:115], v9, s[64:67], s76 offen sc1\n" \
  "buffer_load_dwordx4 v[116:119], v10, s[64:67], s76 offen sc1\n" \
  "v_mfma_f32_16x16x32_bf16 v[48:51], v[16:19], v[132:135], v[48:51]\n" \
  "ds_read_b128 v[16:19], v5 offset:25600\n" \
  "s_add_u32 s74, s74, 0x1000\n" \
  "v_mfma_f32_16x16x32_bf16 v[48:51], v[20:23], v[140:143], v[48:51]\n" \
  "buffer_store_dwordx4 v[36:39], v8, s[64:67], s74 offen offset:0 sc1\n" \
  "s_waitcnt lgkmcnt(2)\n" \
  "v_mfma_f32_16x16x32_bf16 v[52:55], v[24:27], v[128:131], v[52:55]\n" \
  "ds_read_b128 v[20:23], v5 offset:23552\n" \
  "v_mfma_f32_16x16x32_bf16 v[52:55], v[28:31], v[136:139], v[52:55]\n" \
  "v_mfma_f32_16x16x32_bf16 v[56:59], v[24:27], v[132:135], v[56:59]\n" \
  "ds_read_b128 v[24:27], v5 offset:27648\n" \
  "v_mfma_f32_16x16x32_bf16 v[56:59], v[28:31], v[140:143], v[56:59]\n" \
  "s_waitcnt lgkmcnt(2)\n" \
  "v_mfma_f32_16x16x32_bf16 v[92:95], v[32:35], v[128:131], v[92:95]\n" \
  "ds_read_b128 v[28:31], v5 offset:28672\n" \
  "s_setprio 0\n" \
  "v_mfma_f32_16x16x32_bf16 v[92:95], v[16:19], v[136:139], v[92:95]\n" \
  "v_mfma_f32_16x16x32_bf16 v[96:99], v[32:35], v[132:135], v[96:99]\n" \
  "ds_read_b128 v[32:35], v5 offset:32768\n" \
  "v_mfma_f32_16x16x32_bf16 v[96:99], v[16:19], v[140:143], v[96:99]\n" \
  "s_add_u32 s76, s73, 8192\n" \
  "buffer_load_dwordx4 v[120:123], v8, s[64:67], s76 offen offset:0 sc1\n" \
  "buffer_load_dwordx4 v[124:127], v8, s[64:67], s76 offen offset:1024 sc1\n" \
  "s_waitcnt lgkmcnt(2)\n" \
  "v_mfma_f32_16x16x32_bf16 v[100:103], v[20:23], v[128:131], v[100:103]\n" \
  "ds_read_b128 v[16:19], v5 offset:30720\n" \
  "v_mfma_f32_16x16x32_bf16 v[100:103], v[24:27], v[136:139], v[100:103]\n" \
  "v_mfma_f32_16x16x32_bf16 v[104:107], v[20:23], v[132:135], v[104:107]\n" \
  "ds_read_b128 v[20:23], v5 offset:34816\n" \
  "v_mfma_f32_16x16x32_bf16 v[104:107], v[24:27], v[140:143], v[104:107]\n" \
  "s_waitcnt lgkmcnt(2)\n" \
  "v_mfma_f32_16x16x32_bf16 v[44:47], v[28:31], v[144:147], v[44:47]\n" \
  "s_barrier\n" \
  "ds_read_b128 v[24:27], v5 offset:29696\n" \
  "s_setprio 3\n" \
  "v_mfma_f32_16x16x32_bf16 v[44:47], v[32:35], v[152:155], v[44:47]\n" \
  "v_mfma_f32_16x16x32_bf16 v[48:51], v[28:31], v[148:151], v[48:51]\n" \
  "ds_read_b128 v[28:31], v5 offset:33792\n" \
  "v_mfma_f32_16x16x32_bf16 v[48:51], v[32:35], v[156:159], v[48:51]\n" \
  "s_waitcnt lgkmcnt(2)\n" \
  "v_mfma_f32_16x16x32_bf16 v[52:55], v[16:19], v[144:147], v[52:55]\n" \
  "ds_read_b128 v[32:35], v5 offset:31744\n" \
  "v_mfma_f32_16x16x32_bf16 v[52:55], v[20:23], v[152:155], v[52:55]\n" \
  "v_mfma_f32_16x16x32_bf16 v[56:59], v[16:19], v[148:151], v[56:59]\n" \
  "ds_read_b128 v[16:19], v5 offset:35840\n" \
  "v_mfma_f32_16x16x32_bf16 v[56:59], v[20:23], v[156:159], v[56:59]\n" \
  "s_add_u32 m0, s69, 90112\n" \
  "s_add_u32 s68, s68, 0x2000\n" \
  "buffer_load_dwordx4 v8, s[64:67], s68 offen sc1 lds\n" \
  "s_waitcnt lgkmcnt(2)\n" \
  "v_mfma_f32_16x16x32_bf16 v[92:95], v[24:27], v[144:147], v[92:95]\n" \
  "ds_read_b128 v[20:23], v5 offset:36864\n" \
  "v_mfma_f32_16x16x32_bf16 v[92:95], v[28:31], v[152:155], v[92:95]\n" \
  "v_mfma_f32_16x16x32_bf16 v[96:99], v[24:27], v[148:151], v[96:99]\n" \
  "ds_read_b128 v[24:27], v5 offset:40960\n" \
  "v_mfma_f32_16x16x32_bf16 v[96:99], v[28:31], v[156:159], v[96:99]\n" \
  "s_waitcnt lgkmcnt(2)\n" \
  "v_mfma_f32_16x16x32_bf16 v[100:103], v[32:35], v[144:147], v[100:103]\n" \
  "ds_read_b128 v[28:31], v5 offset:38912\n" \
  "v_mfma_f32_16x16x32_bf16 v[100:103], v[16:19], v[152:155], v[100:103]\n" \
  "s_add_u32 s76, s73, 10240\n" \
  "buffer_load_dwordx4 v[128:131], v9, s[64:67], s76 offen sc1\n" \
  "buffer_load_dwordx4 v[132:135], v10, s[64:67], s76 offen sc1\n" \
  "v_mfma_f32_16x16x32_bf16 v[104:107], v[32:35], v[148:151], v[104:107]\n" \
  "ds_read_b128 v[32:35], v5 offset:43008\n" \
  "v_mfma_f32_16x16x32_bf16 v[104:107], v[16:19], v[156:159], v[104:107]\n" \
  "s_waitcnt lgkmcnt(2)\n" \
  "v_mfma_f32_16x16x32_bf16 v[44:47], v[20:23], v[160:163], v[44:47]\n" \
  "ds_read_b128 v[16:19], v5 offset:37888\n" \
  "v_mfma_f32_16x16x32_bf16 v[44:47], v[24:27], v[168:171], v[44:47]\n" \
  "v_mfma_f32_16x16x32_bf16 v[48:51], v[20:23], v[164:167], v[48:51]\n" \
  "ds_read_b128 v[20:23], v5 offset:41984\n" \
  "buffer_store_dwordx4 v[40:43], v8, s[64:67], s74 offen offset:1024 sc1\n" \
  "v_mfma_f32_16x16x32_bf16 v[48:51], v[24:27], v[172:175], v[48:51]\n" \
  "s_waitcnt lgkmcnt(2)\n" \
  "v_mfma_f32_16x16x32_bf16 v[52:55], v[28:31], v[160:163], v[52:55]\n" \
  "ds_read_b128 v[24:27], v5 offset:39936\n" \
  "v_mfma_f32_16x16x32_bf16 v[52:55], v[32:35], v[168:171], v[52:55]\n" \
  "v_mfma_f32_16x16x32_bf16 v[56:59], v[28:31], v[164:167], v[56:59]\n" \
  "ds_read_b128 v[28:31], v5 offset:44032\n" \
  "v_mfma_f32_16x16x32_bf16 v[56:59], v[32:35], v[172:175], v[56:59]\n" \
  "s_waitcnt lgkmcnt(2)\n" \
  "v_mfma_f32_16x16x32_bf16 v[92:95], v[16:19], v[160:163], v[92:95]\n" \
  "ds_read_b128 v[32:35], v5 offset:45056\n" \
  "v_mfma_f32_16x16x32_bf16 v[92:95], v[20:23], v[168:171], v[92:95]\n" \
  "s_add_u32 s76, s73, 18432\n" \
  "buffer_load_dwordx4 v[136:139], v9, s[64:67], s76 offen sc1\n" \
  "buffer_load_dwordx4 v[140:143], v10, s[64:67], s76 offen sc1\n" \
  "v_mfma_f32_16x16x32_bf16 v[96:99], v[16:19], v[164:167], v[96:99]\n" \
  "ds_read_b128 v[16:19], v8 offset:0\n" \
  "v_mfma_f32_16x16x32_bf16 v[96:99], v[20:23], v[172:175], v[96:99]\n" \
  "s_waitcnt lgkmcnt(2)\n" \
  "v_mfma_f32_16x16x32_bf16 v[100:103], v[24:27], v[160:163], v[100:103]\n" \
  "ds_read_b128 v[20:23], v5 offset:47104\n" \
  "s_setprio 2\n" \
  "v_mfma_f32_16x16x32_bf16 v[100:103], v[28:31], v[168:171], v[100:103]\n" \
  "v_mfma_f32_16x16x32_bf16 v[104:107], v[24:27], v[164:167], v[104:107]\n" \
  "ds_read_b128 v[24:27], v8 offset:2048\n" \
  "v_mfma_f32_16x16x32_bf16 v[104:107], v[28:31], v[172:175], v[104:107]\n" \
  "s_waitcnt lgkmcnt(2)\n" \
  "v_mfma_f32_16x16x32_bf16 v[44:47], v[32:35], v[176:179], v[44:47]\n" \
  "ds_read_b128 v[28:31], v5 offset:46080\n" \
  "v_mfma_f32_16x16x32_bf16 v[44:47], v[16:19], v[184:187], v[44:47]\n" \
  "v_mfma_f32_16x16x32_bf16 v[48:51], v[32:35], v[180:183], v[48:51]\n" \
  "ds_read_b128 v[32:35], v8 offset:1024\n" \
  "v_mfma_f32_16x16x32_bf16 v[48:51], v[16:19], v[188:191], v[48:51]\n" \
  "s_waitcnt lgkmcnt(2)\n" \
  "v_mfma_f32_16x16x32_bf16 v[52:55], v[20:23], v[176:179], v[52:55]\n" \
  "ds_read_b128 v[16:19], v5 offset:48128\n" \
  "v_mfma_f32_16x16x32_bf16 v[52:55], v[24:27], v[184:187], v[52:55]\n" \
  "s_add_u32 m0, s69, 98304\n" \
  "s_add_u32 s68, s68, 0x2000\n" \
  "buffer_load_dwordx4 v8, s[64:67], s68 offen sc1 lds\n" \
  "v_mfma_f32_16x16x32_bf16 v[56:59], v[20:23], v[180:183], v[56:59]\n" \
  "ds_read_b128 v[20:23], v8 offset:3072\n" \
  "v_mfma_f32_16x16x32_bf16 v[56:59], v[24:27], v[188:191], v[56:59]\n" \
  "s_waitcnt lgkmcnt(2)\n" \
  "v_mfma_f32_16x16x32_bf16 v[92:95], v[28:31], v[176:179], v[92:95]\n" \
  "ds_read_b128 v[24:27], v8 offset:4096\n" \
  "v_mfma_f32_16x16x32_bf16 v[92:95], v[32:35], v[184:187], v[92:95]\n" \
  "v_mfma_f32_16x16x32_bf16 v[96:99], v[28:31], v[180:183], v[96:99]\n" \
  "ds_read_b128 v[28:31], v8 offset:8192\n" \
  "v_mfma_f32_16x16x32_bf16 v[96:99], v[32:35], v[188:191], v[96:99]\n" \
  "s_add_u32 m0, s69, 106496\n" \
  "s_add_u32 s68, s68, 0x2000\n" \
  "buffer_load_dwordx4 v8, s[64:67], s68 offen sc1 lds\n" \
  "s_waitcnt lgkmcnt(2)\n" \
  "v_mfma_f32_16x16x32_bf16 v[100:103], v[16:19], v[176:179], v[100:103]\n" \
  "ds_read_b128 v[32:35], v8 offset:6144\n" \
  "v_mfma_f32_16x16x32_bf16 v[100:103], v[20:23], v[184:187], v[100:103]\n" \
  "v_mfma_f32_16x16x32_bf16 v[104:107], v[16:19], v[180:183], v[104:107]\n" \
  "ds_read_b128 v[16:19], v8 offset:10240\n" \
  "v_mfma_f32_16x16x32_bf16 v[104:107], v[20:23], v[188:191], v[104:107]\n" \
  "s_waitcnt lgkmcnt(2)\n" \
  "v_mfma_f32_16x16x32_bf16 v[44:47], v[24:27], v[192:195], v[44:47]\n" \
  "ds_read_b128 v[20:23], v8 offset:5120\n" \
  "s_setprio 1\n" \
  "v_mfma_f32_16x16x32_bf16 v[44:47], v[28:31], v[200:203], v[44:47]\n" \
  "s_add_u32 m0, s69, 114688\n" \
  "s_add_u32 s68, s68, 0x2000\n" \
  "buffer_load_dwordx4 v8, s[64:67], s68 offen sc1 lds\n" \
  "v_mfma_f32_16x16x32_bf16 v[48:51], v[24:27], v[196:199], v[48:51]\n" \
  "ds_read_b128 v[24:27], v8 offset:9216\n" \
  "v_mfma_f32_16x16x32_bf16 v[48:51], v[28:31], v[204:207], v[48:51]\n" \
  "s_waitcnt lgkmcnt(2)\n" \
  "v_mfma_f32_16x16x32_bf16 v[52:55], v[32:35], v[192:195], v[52:55]\n" \
  "ds_read_b128 v[28:31], v8 offset:7168\n" \
  "v_mfma_f32_16x16x32_bf16 v[52:55], v[16:19], v[200:203], v[52:55]\n" \
  "v_mfma_f32_16x16x32_bf16 v[56:59], v[32:35], v[196:199], v[56:59]\n" \
  "ds_read_b128 v[32:35], v8 offset:11264\n" \
  "v_mfma_f32_16x16x32_bf16 v[56:59], v[16:19], v[204:207], v[56:59]\n" \
  "s_add_u32 s76, s73, 26624\n" \
  "buffer_load_dwordx4 v[144:147], v8, s[64:67], s76 offen offset:0 sc1\n" \
  "buffer_load_dwordx4 v[148:151], v8, s[64:67], s76 offen offset:1024 sc1\n" \
  "s_waitcnt lgkmcnt(2)\n" \
  "v_mfma_f32_16x16x32_bf16 v[92:95], v[20:23], v[192:195], v[92:95]\n" \
  "ds_read_b128 v[16:19], v8 offset:12288\n" \
  "s_setprio 0\n" \
  "v_mfma_f32_16x16x32_bf16 v[92:95], v[24:27], v[200:203], v[92:95]\n" \
  "v_mfma_f32_16x16x32_bf16 v[96:99], v[20:23], v[196:199], v[96:99]\n" \
  "ds_read_b128 v[20:23], v8 offset:16384\n" \
  "v_mfma_f32_16x16x32_bf16 v[96:99], v[24:27], v[204:207], v[96:99]\n" \
  "s_waitcnt lgkmcnt(2)\n" \
  "v_mfma_f32_16x16x32_bf16 v[100:103], v[28:31], v[192:195], v[100:103]\n" \
  "ds_read_b128 v[24:27], v8 offset:14336\n" \
  "v_mfma_f32_16x16x32_bf16 v[100:103], v[32:35], v[200:203], v[100:103]\n" \
  "v_mfma_f32_16x16x32_bf16 v[104:107], v[28:31], v[196:199], v[104:107]\n" \
  "ds_read_b128 v[28:31], v8 offset:18432\n" \
  "v_mfma_f32_16x16x32_bf16 v[104:107], v[32:35], v[204:207], v[104:107]\n" \
  "s_waitcnt lgkmcnt(2)\n" \
  "v_mfma_f32_16x16x32_bf16 v[44:47], v[16:19], v[208:211], v[44:47]\n" \
  "s_barrier\n" \
  "ds_read_b128 v[32:35], v8 offset:13312\n" \
  "s_setprio 3\n" \
  "v_mfma_f32_16x16x32_bf16 v[44:47], v[20:23], v[216:219], v[44:47]\n" \
  "v_mfma_f32_16x16x32_bf16 v[48:51], v[16:19], v[212:215], v[48:51]\n" \
  "ds_read_b128 v[16:19], v8 offset:17408\n" \
  "v_mfma_f32_16x16x32_bf16 v[48:51], v[20:23], v[220:223], v[48:51]\n" \
  "s_add_u32 m0, s69, 122880\n" \
  "s_add_u32 s68, s68, 0x2000\n" \
  "buffer_load_dwordx4 v8, s[64:67], s68 offen sc1 lds\n" \
  "s_waitcnt lgkmcnt(2)\n" \
  "v_mfma_f32_16x16x32_bf16 v[52:55], v[24:27], v[208:211], v[52:55]\n" \
  "ds_read_b128 v[20:23], v8 offset:15360\n" \
  "v_mfma_f32_16x16x32_bf16 v[52:55], v[28:31], v[216:219], v[52:55]\n" \
  "s_waitcnt vmcnt(8)\n" \
  "v_mov_b32 v7, 2\n" \
  "s_mov_b64 exec, 1\n" \
  "buffer_store_dword v7, v8, s[64:67], s77 offen sc1\n" \
  "s_mov_b64 exec, -1\n" \
  "v_mfma_f32_16x16x32_bf16 v[56:59], v[24:27], v[212:215], v[56:59]\n" \
  "ds_read_b128 v[24:27], v8 offset:19456\n" \
  "v_mfma_f32_16x16x32_bf16 v[56:59], v[28:31], v[220:223], v[56:59]\n" \
  "s_waitcnt lgkmcnt(2)\n" \
  "v_mfma_f32_16x16x32_bf16 v[92:95], v[32:35], v[208:211], v[92:95]\n" \
  "ds_read_b128 v[28:31], v8 offset:20480\n" \
  "v_mfma_f32_16x16x32_bf16 v[92:95], v[16:19], v[216:219], v[92:95]\n" \
  "s_add_u32 m0, s69, 131072\n" \
  "s_add_u32 s68, s68, 0x2000\n" \
  "buffer_load_dwordx4 v8, s[64:67], s68 offen sc1 lds\n" \
  "v_mfma_f32_16x16x32_bf16 v[96:99], v[32:35], v[212:215], v[96:99]\n" \
  "ds_read_b128 v[32:35], v8 offset:24576\n" \
  "v_mfma_f32_16x16x32_bf16 v[96:99], v[16:19], v[220:223], v[96:99]\n" \
  "s_waitcnt lgkmcnt(2)\n" \
  "v_mfma_f32_16x16x32_bf16 v[100:103], v[20:23], v[208:211], v[100:103]\n" \
  "ds_read_b128 v[16:19], v8 offset:22528\n" \
  "v_mfma_f32_16x16x32_bf16 v[100:103], v[24:27], v[216:219], v[100:103]\n" \
  "v_mfma_f32_16x16x32_bf16 v[104:107], v[20:23], v[212:215], v[104:107]\n" \
  "ds_read_b128 v[20:23], v8 offset:26624\n" \
  "v_mfma_f32_16x16x32_bf16 v[104:107], v[24:27], v[220:223], v[104:107]\n" \
  "s_add_u32 m0, s69, 139264\n" \
  "s_add_u32 s68, s68, 0x2000\n" \
  "buffer_load_dwordx4 v8, s[64:67], s68 offen sc1 lds\n" \
  "s_waitcnt lgkmcnt(2)\n" \
  "v_mfma_f32_16x16x32_bf16 v[44:47], v[28:31], v[224:227], v[44:47]\n" \
  "ds_read_b128 v[24:27], v8 offset:21504\n" \
  "v_mfma_f32_16x16x32_bf16 v[44:47], v[32:35], v[232:235], v[44:47]\n" \
  "v_mfma_f32_16x16x32_bf16 v[48:51], v[28:31], v[228:231], v[48:51]\n" \
  "ds_read_b128 v[28:31], v8 offset:25600\n" \
  "v_mfma_f32_16x16x32_bf16 v[48:51], v[32:35], v[236:239], v[48:51]\n" \
  "s_waitcnt lgkmcnt(2)\n" \
  "v_mfma_f32_16x16x32_bf16 v[52:55], v[16:19], v[224:227], v[52:55]\n" \
  "ds_read_b128 v[32:35], v8 offset:23552\n" \
  "s_setprio 2\n" \
  "v_mfma_f32_16x16x32_bf16 v[52:55], v[20:23], v[232:235], v[52:55]\n" \
  "s_add_u32 s76, s73, 28672\n" \
  "buffer_load_dwordx4 v[152:155], v9, s[64:67], s76 offen sc1\n" \
  "buffer_load_dwordx4 v[156:159], v10, s[64:67], s76 offen sc1\n" \
  "v_mfma_f32_16x16x32_bf16 v[56:59], v[16:19], v[228:231], v[56:59]\n" \
  "ds_read_b128 v[16:19], v8 offset:27648\n" \
  "v_mfma_f32_16x16x32_bf16 v[56:59], v[20:23], v[236:239], v[56:59]\n" \
  "s_waitcnt lgkmcnt(2)\n" \
  "v_mfma_f32_16x16x32_bf16 v[92:95], v[24:27], v[224:227], v[92:95]\n" \
  "ds_read_b128 v[20:23], v8 offset:28672\n" \
  "v_mfma_f32_16x16x32_bf16 v[92:95], v[28:31], v[232:235], v[92:95]\n" \
  "v_mfma_f32_16x16x32_bf16 v[96:99], v[24:27], v[228:231], v[96:99]\n" \
  "ds_read_b128 v[24:27], v8 offset:32768\n" \
  "v_mfma_f32_16x16x32_bf16 v[96:99], v[28:31], v[236:239], v[96:99]\n" \
  "s_waitcnt lgkmcnt(2)\n" \
  "v_mfma_f32_16x16x32_bf16 v[100:103], v[32:35], v[224:227], v[100:103]\n" \
  "ds_read_b128 v[28:31], v8 offset:30720\n" \
  "v_mfma_f32_16x16x32_bf16 v[100:103], v[16:19], v[232:235], v[100:103]\n" \
  "v_mfma_f32_16x16x32_bf16 v[104:107], v[32:35], v[228:231], v[104:107]\n" \
  "ds_read_b128 v[32:35], v8 offset:34816\n" \
  "v_mfma_f32_16x16x32_bf16 v[104:107], v[16:19], v[236:239], v[104:107]\n" \
  "s_waitcnt lgkmcnt(2)\n" \
  "v_mfma_f32_16x16x32_bf16 v[44:47], v[20:23], v[240:243], v[44:47]\n" \
  "ds_read_b128 v[16:19], v8 offset:29696\n" \
  "v_mfma_f32_16x16x32_bf16 v[44:47], v[24:27], v[248:251], v[44:47]\n" \
  "s_add_u32 s76, s73, 2048\n" \
  "buffer_load_dwordx4 v[160:163], v9, s[64:67], s76 offen sc1\n" \
  "buffer_load_dwordx4 v[164:167], v10, s[64:67], s76 offen sc1\n" \
  "v_mfma_f32_16x16x32_bf16 v[48:51], v[20:23], v[244:247], v[48:51]\n" \
  "ds_read_b128 v[20:23], v8 offset:33792\n" \
  "v_mfma_f32_16x16x32_bf16 v[48:51], v[24:27], v[252:255], v[48:51]\n" \
  "s_waitcnt lgkmcnt(2)\n" \
  "v_mfma_f32_16x16x32_bf16 v[52:55], v[28:31], v[240:243], v[52:55]\n" \
  "ds_read_b128 v[24:27], v8 offset:31744\n" \
  "s_setprio 1\n" \
  "v_mfma_f32_16x16x32_bf16 v[52:55], v[32:35], v[248:251], v[52:55]\n" \
  "v_mfma_f32_16x16x32_bf16 v[56:59], v[28:31], v[244:247], v[56:59]\n" \
  "ds_read_b128 v[28:31], v8 offset:35840\n" \
  "v_mfma_f32_16x16x32_bf16 v[56:59], v[32:35], v[252:255], v[56:59]\n" \
  "s_waitcnt lgkmcnt(2)\n" \
  "v_mfma_f32_16x16x32_bf16 v[92:95], v[16:19], v[240:243], v[92:95]\n" \
  "v_add_u32 v4, s79, v4\n" \
  "v_add_u32 v5, s79, v5\n" \
  "ds_read_b128 v[32:35], v3 offset:36864\n" \
  "v_mfma_f32_16x16x32_bf16 v[92:95], v[20:23], v[248:251], v[92:95]\n" \
  "v_mfma_f32_16x16x32_bf16 v[96:99], v[16:19], v[244:247], v[96:99]\n" \
  "ds_read_b128 v[16:19], v3 offset:38912\n" \
  "s_setprio 0\n" \
  "v_mfma_f32_16x16x32_bf16 v[96:99], v[20:23], v[252:255], v[96:99]\n" \
  "s_waitcnt lgkmcnt(2)\n" \
  "v_mfma_f32_16x16x32_bf16 v[100:103], v[24:27], v[240:243], v[100:103]\n" \
  "ds_read_b128 v[20:23], v3 offset:40960\n" \
  "v_mfma_f32_16x16x32_bf16 v[100:103], v[28:31], v[248:251], v[100:103]\n" \
  "s_add_u32 s76, s73, 12288\n" \
  "buffer_load_dwordx4 v[168:171], v9, s[64:67], s76 offen sc1\n" \
  "buffer_load_dwordx4 v[172:175], v10, s[64:67], s76 offen sc1\n" \
  "v_swap_b32 v122, v124\n" \
  "v_swap_b32 v123, v125\n" \
  "v_mfma_f32_16x16x32_bf16 v[104:107], v[24:27], v[244:247], v[104:107]\n" \
  "ds_read_b128 v[24:27], v3 offset:43008\n" \
  "v_mul_f32 v44, v44, v52\n" \
  "v_mfma_f32_16x16x32_bf16 v[104:107], v[28:31], v[252:255], v[104:107]\n" \
  "v_mul_f32 v45, v45, v53\n" \
  "s_waitcnt lgkmcnt(2)\n" \
  "v_mfma_f32_16x16x32_bf16 v[60:63], v[32:35], v[112:115], 0\n" \
  "s_barrier\n" \
  "ds_read_b128 v[28:31], v3 offset:45056\n" \
  "s_setprio 3\n" \
  "v_mul_f32 v46, v46, v54\n" \
  "v_mfma_f32_16x16x32_bf16 v[60:63], v[16:19], v[120:123], v[60:63]\n" \
  "v_mul_f32 v47, v47, v55\n" \
  "v_mfma_f32_16x16x32_bf16 v[64:67], v[32:35], v[116:119], 0\n" \
  "ds_read_b128 v[32:35], v3 offset:47104\n" \
  "v_cvt_pk_bf16_f32 v36, v44, v45\n" \
  "v_mfma_f32_16x16x32_bf16 v[64:67], v[16:19], v[124:127], v[64:67]\n" \
  "v_cvt_pk_bf16_f32 v37, v46, v47\n" \
  "s_waitcnt lgkmcnt(2)\n" \
  "v_mfma_f32_16x16x32_bf16 v[68:71], v[20:23], v[112:115], 0\n" \
  "ds_read_b128 v[16:19], v4 offset:0\n" \
  "v_mul_f32 v48, v48, v56\n" \
  "buffer_load_dword v6, v12, s[64:67], 0 offen sc1\n" \
  "v_mfma_f32_16x16x32_bf16 v[68:71], v[24:27], v[120:123], v[68:71]\n" \
  "v_mul_f32 v49, v49, v57\n" \
  "v_mfma_f32_16x16x32_bf16 v[72:75], v[20:23], v[116:119], 0\n" \
  "ds_read_b128 v[20:23], v4 offset:2048\n" \
  "v_mul_f32 v50, v50, v58\n" \
  "v_mfma_f32_16x16x32_bf16 v[72:75], v[24:27], v[124:127], v[72:75]\n" \
  "v_mul_f32 v51, v51, v59\n" \
  "s_add_u32 m0, s69, 0\n" \
  "s_add_u32 s68, s68, 0x2000\n" \
  "buffer_load_dwordx4 v8, s[64:67], s68 offen sc1 lds\n" \
  "s_waitcnt lgkmcnt(2)\n" \
  "v_mfma_f32_16x16x32_bf16 v[76:79], v[28:31], v[112:115], 0\n" \
  "ds_read_b128 v[24:27], v4 offset:4096\n" \
  "v_cvt_pk_bf16_f32 v40, v48, v49\n" \
  "v_mfma_f32_16x16x32_bf16 v[76:79], v[32:35], v[120:123], v[76:79]\n" \
  "v_cvt_pk_bf16_f32 v41, v50, v51\n" \
  "v_mfma_f32_16x16x32_bf16 v[80:83], v[28:31], v[116:119], 0\n" \
  "ds_read_b128 v[28:31], v4 offset:6144\n" \
  "v_mul_f32 v92, v92, v100\n" \
  "v_mfma_f32_16x16x32_bf16 v[80:83], v[32:35], v[124:127], v[80:83]\n" \
  "v_mul_f32 v93, v93, v101\n" \
  "s_waitcnt lgkmcnt(2)\n" \
  "v_mfma_f32_16x16x32_bf16 v[84:87], v[16:19], v[112:115], 0\n" \
  "ds_read_b128 v[32:35], v4 offset:8192\n" \
  "v_mul_f32 v94, v94, v102\n" \
  "v_mfma_f32_16x16x32_bf16 v[84:87], v[20:23], v[120:123], v[84:87]\n" \
  "v_mul_f32 v95, v95, v103\n" \
  "s_add_u32 m0, s69, 8192\n" \
  "s_add_u32 s68, s68, 0x2000\n" \
  "buffer_load_dwordx4 v8, s[64:67], s68 offen sc1 lds\n" \
  "v_mfma_f32_16x16x32_bf16 v[88:91], v[16:19], v[116:119], 0\n" \
  "ds_read_b128 v[16:19], v4 offset:10240\n" \
  "v_cvt_pk_bf16_f32 v38, v92, v93\n" \
  "v_mfma_f32_16x16x32_bf16 v[88:91], v[20:23], v[124:127], v[88:91]\n" \
  "v_cvt_pk_bf16_f32 v39, v94, v95\n" \
  "s_waitcnt lgkmcnt(2)\n" \
  "v_mfma_f32_16x16x32_bf16 v[44:47], v[24:27], v[112:115], 0\n" \
  "ds_read_b128 v[20:23], v4 offset:12288\n" \
  "v_mul_f32 v96, v96, v104\n" \
  "v_mfma_f32_16x16x32_bf16 v[44:47], v[28:31], v[120:123], v[44:47]\n" \
  "v_mul_f32 v97, v97, v105\n" \
  "s_add_u32 m0, s69, 16384\n" \
  "s_add_u32 s68, s68, 0x2000\n" \
  "buffer_load_dwordx4 v8, s[64:67], s68 offen sc1 lds\n" \
  "v_mfma_f32_16x16x32_bf16 v[48:51], v[24:27], v[116:119], 0\n" \
  "ds_read_b128 v[24:27], v4 offset:14336\n" \
  "v_mul_f32 v98, v98, v106\n" \
  "v_mfma_f32_16x16x32_bf16 v[48:51], v[28:31], v[124:127], v[48:51]\n" \
  "v_mul_f32 v99, v99, v107\n" \
  "s_waitcnt lgkmcnt(2)\n" \
  "v_mfma_f32_16x16x32_bf16 v[52:55], v[32:35], v[112:115], 0\n" \
  "ds_read_b128 v[28:31], v4 offset:16384\n" \
  "s_setprio 2\n" \
  "v_cvt_pk_bf16_f32 v42, v96, v97\n" \
  "v_mfma_f32_16x16x32_bf16 v[52:55], v[16:19], v[120:123], v[52:55]\n" \
  "v_cvt_pk_bf16_f32 v43, v98, v99\n" \
  "v_mfma_f32_16x16x32_bf16 v[56:59], v[32:35], v[116:119], 0\n" \
  "ds_read_b128 v[32:35], v4 offset:18432\n" \
  "buffer_store_dwordx4 v[36:39], v8, s[64:67], s74 offen offset:2048 sc1\n" \
  "v_mfma_f32_16x16x32_bf16 v[56:59], v[16:19], v[124:127], v[56:59]\n" \
  "s_add_u32 m0, s69, 24576\n" \
  "s_add_u32 s68, s68, 0x2000\n" \
  "buffer_load_dwordx4 v8, s[64:67], s68 offen sc1 lds\n" \
  "s_waitcnt lgkmcnt(2)\n" \
  "v_mfma_f32_16x16x32_bf16 v[92:95], v[20:23], v[112:115], 0\n" \
  "ds_read_b128 v[16:19], v4 offset:20480\n" \
  "v_mfma_f32_16x16x32_bf16 v[92:95], v[24:27], v[120:123], v[92:95]\n" \
  "v_mfma_f32_16x16x32_bf16 v[96:99], v[20:23], v[116:119], 0\n" \
  "ds_read_b128 v[20:23], v4 offset:22528\n" \
  "v_mfma_f32_16x16x32_bf16 v[96:99], v[24:27], v[124:127], v[96:99]\n" \
  "s_add_u32 s76, s73, 20480\n" \
  "buffer_load_dwordx4 v[176:179], v9, s[64:67], s76 offen sc1\n" \
  "buffer_load_dwordx4 v[180:183], v10, s[64:67], s76 offen sc1\n" \
  "s_waitcnt vmcnt(23)\n" \
  "s_waitcnt lgkmcnt(2)\n" \
  "v_mfma_f32_16x16x32_bf16 v[60:63], v[28:31], v[128:131], v[60:63]\n" \
  "ds_read_b128 v[24:27], v4 offset:24576\n" \
  "s_waitcnt vmcnt(7)\n" \
  "v_cmp_gt_u32 vcc, 2, v6\n" \
  "s_cbranch_vccnz 42f\n" \
  "32:\n" \
  "v_mfma_f32_16x16x32_bf16 v[60:63], v[32:35], v[136:139], v[60:63]\n" \
  "v_mfma_f32_16x16x32_bf16 v[64:67], v[28:31], v[132:135], v[64:67]\n" \
  "ds_read_b128 v[28:31], v4 offset:26624\n" \
  "v_mfma_f32_16x16x32_bf16 v[64:67], v[32:35], v[140:143], v[64:67]\n" \
  "s_waitcnt lgkmcnt(2)\n" \
  "v_mfma_f32_16x16x32_bf16 v[68:71], v[16:19], v[128:131], v[68:71]\n" \
  "ds_read_b128 v[32:35], v4 offset:28672\n" \
  "v_mfma_f32_16x16x32_bf16 v[68:71], v[20:23], v[136:139], v[68:71]\n" \
  "v_mfma_f32_16x16x32_bf16 v[72:75], v[16:19], v[132:135], v[72:75]\n" \
  "ds_read_b128 v[16:19], v4 offset:30720\n" \
  "v_mfma_f32_16x16x32_bf16 v[72:75], v[20:23], v[140:143], v[72:75]\n" \
  "buffer_store_dwordx4 v[40:43], v8, s[64:67], s74 offen offset:3072 sc1\n" \
  "s_waitcnt lgkmcnt(2)\n" \
  "v_mfma_f32_16x16x32_bf16 v[76:79], v[24:27], v[128:131], v[76:79]\n" \
  "ds_read_b128 v[20:23], v4 offset:32768\n" \
  "s_setprio 1\n" \
  "v_mfma_f32_16x16x32_bf16 v[76:79], v[28:31], v[136:139], v[76:79]\n" \
  "s_add_u32 s76, s73, 30720\n" \
  "buffer_load_dwordx4 v[184:187], v9, s[64:67], s76 offen sc1\n" \
  "buffer_load_dwordx4 v[188:191], v10, s[64:67], s76 offen sc1\n" \
  "v_mfma_f32_16x16x32_bf16 v[80:83], v[24:27], v[132:135], v[80:83]\n" \
  "ds_read_b128 v[24:27], v4 offset:34816\n" \
  "v_mfma_f32_16x16x32_bf16 v[80:83], v[28:31], v[140:143], v[80:83]\n" \
  "s_waitcnt lgkmcnt(2)\n" \
  "v_mfma_f32_16x16x32_bf16 v[84:87], v[32:35], v[128:131], v[84:87]\n" \
  "ds_read_b128 v[28:31], v4 offset:36864\n" \
  "v_mfma_f32_16x16x32_bf16 v[84:87], v[16:19], v[136:139], v[84:87]\n" \
  "v_mfma_f32_16x16x32_bf16 v[88:91], v[32:35], v[132:135], v[88:91]\n" \
  "ds_read_b128 v[32:35], v4 offset:38912\n" \
  "s_setprio 0\n" \
  "v_mfma_f32_16x16x32_bf16 v[88:91], v[16:19], v[140:143], v[88:91]\n" \
  "s_waitcnt lgkmcnt(2)\n" \
  "v_mfma_f32_16x16x32_bf16 v[44:47], v[20:23], v[128:131], v[44:47]\n" \
  "ds_read_b128 v[16:19], v4 offset:40960\n" \
  "v_mfma_f32_16x16x32_bf16 v[44:47], v[24:27], v[136:139], v[44:47]\n" \
  "v_mfma_f32_16x16x32_bf16 v[48:51], v[20:23], v[132:135], v[48:51]\n" \
  "ds_read_b128 v[20:23], v4 offset:43008\n" \
  "v_mfma_f32_16x16x32_bf16 v[48:51], v[24:27], v[140:143], v[48:51]\n" \
  "s_add_u32 s76, s73, 4096\n" \
  "buffer_load_dwordx4 v[192:195], v9, s[64:67], s76 offen sc1\n" \
  "buffer_load_dwordx4 v[196:199], v10, s[64:67], s76 offen sc1\n" \
  "s_waitcnt lgkmcnt(2)\n" \
  "v_mfma_f32_16x16x32_bf16 v[52:55], v[28:31], v[128:131], v[52:55]\n" \
  "s_barrier\n" \
  "ds_read_b128 v[24:27], v4 offset:45056\n" \
  "s_setprio 3\n" \
  "v_mfma_f32_16x16x32_bf16 v[52:55], v[32:35], v[136:139], v[52:55]\n" \
  "v_mfma_f32_16x16x32_bf16 v[56:59], v[28:31], v[132:135], v[56:59]\n" \
  "ds_read_b128 v[28:31], v4 offset:47104\n" \
  "v_mfma_f32_16x16x32_bf16 v[56:59], v[32:35], v[140:143], v[56:59]\n" \
  "s_waitcnt lgkmcnt(2)\n" \
  "v_mfma_f32_16x16x32_bf16 v[92:95], v[16:19], v[128:131], v[92:95]\n" \
  "ds_read_b128 v[32:35], v5 offset:0\n" \
  "v_mfma_f32_16x16x32_bf16 v[92:95], v[20:23], v[136:139], v[92:95]\n" \
  "v_swap_b32 v146, v148\n" \
  "v_swap_b32 v147, v149\n" \
  "v_mfma_f32_16x16x32_bf16 v[96:99], v[16:19], v[132:135], v[96:99]\n" \
  "ds_read_b128 v[16:19], v5 offset:2048\n" \
  "v_mfma_f32_16x16x32_bf16 v[96:99], v[20:23], v[140:143], v[96:99]\n" \
  "s_waitcnt lgkmcnt(2)\n" \
  "v_mfma_f32_16x16x32_bf16 v[60:63], v[24:27], v[144:147], v[60:63]\n" \
  "ds_read_b128 v[20:23], v5 offset:4096\n" \
  "v_mfma_f32_16x16x32_bf16 v[60:63], v[28:31], v[152:155], v[60:63]\n" \
  "s_add_u32 m0, s69, 32768\n" \
  "s_add_u32 s68, s68, 0x2000\n" \
  "buffer_load_dwordx4 v8, s[64:67], s68 offen sc1 lds\n" \
  "v_mfma_f32_16x16x32_bf16 v[64:67], v[24:27], v[148:151], v[64:67]\n" \
  "ds_read_b128 v[24:27], v5 offset:6144\n" \
  "v_mfma_f32_16x16x32_bf16 v[64:67], v[28:31], v[156:159], v[64:67]\n" \
  "s_waitcnt lgkmcnt(2)\n" \
  "v_mfma_f32_16x16x32_bf16 v[68:71], v[32:35], v[144:147], v[68:71]\n" \
  "ds_read_b128 v[28:31], v5 offset:8192\n" \
  "v_mfma_f32_16x16x32_bf16 v[68:71], v[16:19], v[152:155], v[68:71]\n" \
  "v_mfma_f32_16x16x32_bf16 v[72:75], v[32:35], v[148:151], v[72:75]\n" \
  "ds_read_b128 v[32:35], v5 offset:10240\n" \
  "v_mfma_f32_16x16x32_bf16 v[72:75], v[16:19], v[156:159], v[72:75]\n" \
  "s_add_u32 s76, s73, 14336\n" \
  "buffer_load_dwordx4 v[200:203], v9, s[64:67], s76 offen sc1\n" \
  "buffer_load_dwordx4 v[204:207], v10, s[64:67], s76 offen sc1\n" \
  "s_waitcnt lgkmcnt(2)\n" \
  "v_mfma_f32_16x16x32_bf16 v[76:79], v[20:23], v[144:147], v[76:79]\n" \
  "ds_read_b128 v[16:19], v5 offset:12288\n" \
  "v_mfma_f32_16x16x32_bf16 v[76:79], v[24:27], v[152:155], v[76:79]\n" \
  "v_mfma_f32_16x16x32_bf16 v[80:83], v[20:23], v[148:151], v[80:83]\n" \
  "ds_read_b128 v[20:23], v5 offset:14336\n" \
  "v_mfma_f32_16x16x32_bf16 v[80:83], v[24:27], v[156:159], v[80:83]\n" \
  "s_waitcnt lgkmcnt(2)\n" \
  "v_mfma_f32_16x16x32_bf16 v[84:87], v[28:31], v[144:147], v[84:87]\n" \
  "ds_read_b128 v[24:27], v5 offset:16384\n" \
  "s_setprio 2\n" \
  "v_mfma_f32_16x16x32_bf16 v[84:87], v[32:35], v[152:155], v[84:87]\n" \
  "v_mfma_f32_16x16x32_bf16 v[88:91], v[28:31], v[148:151], v[88:91]\n" \
  "ds_read_b128 v[28:31], v5 offset:18432\n" \
  "v_mfma_f32_16x16x32_bf16 v[88:91], v[32:35], v[156:159], v[88:91]\n" \
  "s_waitcnt lgkmcnt(2)\n" \
  "v_mfma_f32_16x16x32_bf16 v[44:47], v[16:19], v[144:147], v[44:47]\n" \
  "ds_read_b128 v[32:35], v5 offset:20480\n" \
  "v_mfma_f32_16x16x32_bf16 v[44:47], v[20:23], v[152:155], v[44:47]\n" \
  "s_add_u32 m0, s69, 40960\n" \
  "s_add_u32 s68, s68, 0x2000\n" \
  "buffer_load_dwordx4 v8, s[64:67], s68 offen sc1 lds\n" \
  "v_mfma_f32_16x16x32_bf16 v[48:51], v[16:19], v[148:151], v[48:51]\n" \
  "ds_read_b128 v[16:19], v5 offset:22528\n" \
  "v_mfma_f32_16x16x32_bf16 v[48:51], v[20:23], v[156:159], v[48:51]\n" \
  "s_waitcnt lgkmcnt(2)\n" \
  "v_mfma_f32_16x16x32_bf16 v[52:55], v[24:27], v[144:147], v[52:55]\n" \
  "ds_read_b128 v[20:23], v5 offset:24576\n" \
  "v_mfma_f32_16x16x32_bf16 v[52:55], v[28:31], v[152:155], v[52:55]\n" \
  "s_add_u32 m0, s69, 49152\n" \
  "s_add_u32 s68, s68, 0x2000\n" \
  "buffer_load_dwordx4 v8, s[64:67], s68 offen sc1 lds\n" \
  "v_mfma_f32_16x16x32_bf16 v[56:59], v[24:27], v[148:151], v[56:59]\n" \
  "ds_read_b128 v[24:27], v5 offset:26624\n" \
  "v_mfma_f32_16x16x32_bf16 v[56:59], v[28:31], v[156:159], v[56:59]\n" \
  "s_waitcnt lgkmcnt(2)\n" \
  "v_mfma_f32_16x16x32_bf16 v[92:95], v[32:35], v[144:147], v[92:95]\n" \
  "ds_read_b128 v[28:31], v5 offset:28672\n" \
  "s_waitcnt vmcnt(9)\n" \
  "v_mov_b32 v7, 3\n" \
  "s_mov_b64 exec, 1\n" \
  "buffer_store_dword v7, v8, s[64:67], s77 offen sc1\n" \
  "s_mov_b64 exec, -1\n" \
  "v_mfma_f32_16x16x32_bf16 v[92:95], v[16:19], v[152:155], v[92:95]\n" \
  "v_mfma_f32_16x16x32_bf16 v[96:99], v[32:35], v[148:151], v[96:99]\n" \
  "ds_read_b128 v[32:35], v5 offset:30720\n" \
  "v_mfma_f32_16x16x32_bf16 v[96:99], v[16:19], v[156:159], v[96:99]\n" \
  "s_add_u32 m0, s69, 57344\n" \
  "s_add_u32 s68, s68, 0x2000\n" \
  "buffer_load_dwordx4 v8, s[64:67], s68 offen sc1 lds\n" \
  "s_waitcnt lgkmcnt(2)\n" \
  "v_mfma_f32_16x16x32_bf16 v[60:63], v[20:23], v[160:163], v[60:63]\n" \
  "ds_read_b128 v[16:19], v5 offset:32768\n" \
  "s_setprio 1\n" \
  "v_mfma_f32_16x16x32_bf16 v[60:63], v[24:27], v[168:171], v[60:63]\n" \
  "v_mfma_f32_16x16x32_bf16 v[64:67], v[20:23], v[164:167], v[64:67]\n" \
  "ds_read_b128 v[20:23], v5 offset:34816\n" \
  "v_mfma_f32_16x16x32_bf16 v[64:67], v[24:27], v[172:175], v[64:67]\n" \
  "s_add_u32 m0, s69, 65536\n" \
  "s_add_u32 s68, s68, 0x2000\n" \
  "buffer_load_dwordx4 v8, s[64:67], s68 offen sc1 lds\n" \
  "s_waitcnt lgkmcnt(2)\n" \
  "v_mfma_f32_16x16x32_bf16 v[68:71], v[28:31], v[160:163], v[68:71]\n" \
  "ds_read_b128 v[24:27], v5 offset:36864\n" \
  "v_mfma_f32_16x16x32_bf16 v[68:71], v[32:35], v[168:171], v[68:71]\n" \
  "v_mfma_f32_16x16x32_bf16 v[72:75], v[28:31], v[164:167], v[72:75]\n" \
  "ds_read_b128 v[28:31], v5 offset:38912\n" \
  "s_setprio 0\n" \
  "v_mfma_f32_16x16x32_bf16 v[72:75], v[32:35], v[172:175], v[72:75]\n" \
  "s_waitcnt lgkmcnt(2)\n" \
  "v_mfma_f32_16x16x32_bf16 v[76:79], v[16:19], v[160:163], v[76:79]\n" \
  "ds_read_b128 v[32:35], v5 offset:40960\n" \
  "v_mfma_f32_16x16x32_bf16 v[76:79], v[20:23], v[168:171], v[76:79]\n" \
  "s_add_u32 m0, s69, 73728\n" \
  "s_add_u32 s68, s68, 0x2000\n" \
  "buffer_load_dwordx4 v8, s[64:67], s68 offen sc1 lds\n" \
  "v_mfma_f32_16x16x32_bf16 v[80:83], v[16:19], v[164:167], v[80:83]\n" \
  "ds_read_b128 v[16:19], v5 offset:43008\n" \
  "v_mfma_f32_16x16x32_bf16 v[80:83], v[20:23], v[172:175], v[80:83]\n" \
  "s_waitcnt lgkmcnt(2)\n" \
  "v_mfma_f32_16x16x32_bf16 v[84:87], v[24:27], v[160:163], v[84:87]\n" \
  "s_waitcnt vmcnt(8)\n" \
  "s_barrier\n" \
  "ds_read_b128 v[20:23], v5 offset:45056\n" \
  "s_setprio 3\n" \
  "v_mfma_f32_16x16x32_bf16 v[84:87], v[28:31], v[168:171], v[84:87]\n" \
  "s_add_u32 m0, s69, 81920\n" \
  "s_add_u32 s68, s68, 0x2000\n" \
  "buffer_load_dwordx4 v8, s[64:67], s68 offen sc1 lds\n" \
  "v_mfma_f32_16x16x32_bf16 v[88:91], v[24:27], v[164:167], v[88:91]\n" \
  "ds_read_b128 v[24:27], v5 offset:47104\n" \
  "v_mfma_f32_16x16x32_bf16 v[88:91], v[28:31], v[172:175], v[88:91]\n" \
  "s_waitcnt lgkmcnt(2)\n" \
  "v_mfma_f32_16x16x32_bf16 v[44:47], v[32:35], v[160:163], v[44:47]\n" \
  "ds_read_b128 v[28:31], v3 offset:0\n" \
  "v_mfma_f32_16x16x32_bf16 v[44:47], v[16:19], v[168:171], v[44:47]\n" \
  "v_mfma_f32_16x16x32_bf16 v[48:51], v[32:35], v[164:167], v[48:51]\n" \
  "ds_read_b128 v[32:35], v3 offset:2048\n" \
  "v_mfma_f32_16x16x32_bf16 v[48:51], v[16:19], v[172:175], v[48:51]\n" \
  "s_add_u32 s76, s73, 22528\n" \
  "buffer_load_dwordx4 v[208:211], v9, s[64:67], s76 offen sc1\n" \
  "buffer_load_dwordx4 v[212:215], v10, s[64:67], s76 offen sc1\n" \
  "s_waitcnt lgkmcnt(2)\n" \
  "v_mfma_f32_16x16x32_bf16 v[52:55], v[20:23], v[160:163], v[52:55]\n" \
  "ds_read_b128 v[16:19], v3 offset:4096\n" \
  "v_mfma_f32_16x16x32_bf16 v[52:55], v[24:27], v[168:171], v[52:55]\n" \
  "v_mfma_f32_16x16x32_bf16 v[56:59], v[20:23], v[164:167], v[56:59]\n" \
  "ds_read_b128 v[20:23], v3 offset:6144\n" \
  "v_mfma_f32_16x16x32_bf16 v[56:59], v[24:27], v[172:175], v[56:59]\n" \
  "s_waitcnt lgkmcnt(2)\n" \
  "v_mfma_f32_16x16x32_bf16 v[92:95], v[28:31], v[160:163], v[92:95]\n" \
  "ds_read_b128 v[24:27], v3 offset:8192\n" \
  "v_mfma_f32_16x16x32_bf16 v[92:95], v[32:35], v[168:171], v[92:95]\n" \
  "v_mfma_f32_16x16x32_bf16 v[96:99], v[28:31], v[164:167], v[96:99]\n" \
  "ds_read_b128 v[28:31], v3 offset:10240\n" \
  "v_mfma_f32_16x16x32_bf16 v[96:99], v[32:35], v[172:175], v[96:99]\n" \
  "s_waitcnt lgkmcnt(2)\n" \
  "v_mfma_f32_16x16x32_bf16 v[60:63], v[16:19], v[176:179], v[60:63]\n" \
  "ds_read_b128 v[32:35], v3 offset:12288\n" \
  "v_mfma_f32_16x16x32_bf16 v[60:63], v[20:23], v[184:187], v[60:63]\n" \
  "s_add_u32 s76, s73, 32768\n" \
  "buffer_load_dwordx4 v[216:219], v9, s[64:67], s76 offen sc1\n" \
  "buffer_load_dwordx4 v[220:223], v10, s[64:67], s76 offen sc1\n" \
  "v_mfma_f32_16x16x32_bf16 v[64:67], v[16:19], v[180:183], v[64:67]\n" \
  "ds_read_b128 v[16:19], v3 offset:14336\n" \
  "s_setprio 2\n" \
  "v_mfma_f32_16x16x32_bf16 v[64:67], v[20:23], v[188:191], v[64:67]\n" \
  "s_waitcnt lgkmcnt(2)\n" \
  "v_mfma_f32_16x16x32_bf16 v[68:71], v[24:27], v[176:179], v[68:71]\n" \
  "ds_read_b128 v[20:23], v3 offset:16384\n" \
  "v_mfma_f32_16x16x32_bf16 v[68:71], v[28:31], v[184:187], v[68:71]\n" \
  "v_mfma_f32_16x16x32_bf16 v[72:75], v[24:27], v[180:183], v[72:75]\n" \
  "ds_read_b128 v[24:27], v3 offset:18432\n" \
  "v_mfma_f32_16x16x32_bf16 v[72:75], v[28:31], v[188:191], v[72:75]\n" \
  "s_waitcnt lgkmcnt(2)\n" \
  "v_mfma_f32_16x16x32_bf16 v[76:79], v[32:35], v[176:179], v[76:79]\n" \
  "ds_read_b128 v[28:31], v3 offset:20480\n" \
  "v_mfma_f32_16x16x32_bf16 v[76:79], v[16:19], v[184:187], v[76:79]\n" \
  "v_mfma_f32_16x16x32_bf16 v[80:83], v[32:35], v[180:183], v[80:83]\n" \
  "ds_read_b128 v[32:35], v3 offset:22528\n" \
  "v_mfma_f32_16x16x32_bf16 v[80:83], v[16:19], v[188:191], v[80:83]\n" \
  "s_add_u32 m0, s69, 90112\n" \
  "s_add_u32 s68, s68, 0x2000\n" \
  "buffer_load_dwordx4 v8, s[64:67], s68 offen sc1 lds\n" \
  "s_waitcnt lgkmcnt(2)\n" \
  "v_mfma_f32_16x16x32_bf16 v[84:87], v[20:23], v[176:179], v[84:87]\n" \
  "ds_read_b128 v[16:19], v3 offset:24576\n" \
  "buffer_load_dword v6, v12, s[64:67], 0 offen sc1\n" \
  "v_mfma_f32_16x16x32_bf16 v[84:87], v[24:27], v[184:187], v[84:87]\n" \
  "v_mfma_f32_16x16x32_bf16 v[88:91], v[20:23], v[180:183], v[88:91]\n" \
  "ds_read_b128 v[20:23], v3 offset:26624\n" \
  "v_mfma_f32_16x16x32_bf16 v[88:91], v[24:27], v[188:191], v[88:91]\n" \
  "s_add_u32 m0, s69, 98304\n" \
  "s_add_u32 s68, s68, 0x2000\n" \
  "buffer_load_dwordx4 v8, s[64:67], s68 offen sc1 lds\n" \
  "s_waitcnt lgkmcnt(2)\n" \
  "v_mfma_f32_16x16x32_bf16 v[44:47], v[28:31], v[176:179], v[44:47]\n" \
  "ds_read_b128 v[24:27], v3 offset:28672\n" \
  "s_setprio 1\n" \
  "v_mfma_f32_16x16x32_bf16 v[44:47], v[32:35], v[184:187], v[44:47]\n" \
  "v_mfma_f32_16x16x32_bf16 v[48:51], v[28:31], v[180:183], v[48:51]\n" \
  "ds_read_b128 v[28:31], v3 offset:30720\n" \
  "v_mfma_f32_16x16x32_bf16 v[48:51], v[32:35], v[188:191], v[48:51]\n" \
  "s_waitcnt lgkmcnt(2)\n" \
  "v_mfma_f32_16x16x32_bf16 v[52:55], v[16:19], v[176:179], v[52:55]\n" \
  "ds_read_b128 v[32:35], v3 offset:32768\n" \
  "v_mfma_f32_16x16x32_bf16 v[52:55], v[20:23], v[184:187], v[52:55]\n" \
  "s_add_u32 m0, s69, 106496\n" \
  "s_add_u32 s68, s68, 0x2000\n" \
  "buffer_load_dwordx4 v8, s[64:67], s68 offen sc1 lds\n" \
  "v_mfma_f32_16x16x32_bf16 v[56:59], v[16:19], v[180:183], v[56:59]\n" \
  "ds_read_b128 v[16:19], v3 offset:34816\n" \
  "s_setprio 0\n" \
  "v_mfma_f32_16x16x32_bf16 v[56:59], v[20:23], v[188:191], v[56:59]\n" \
  "s_waitcnt lgkmcnt(2)\n" \
  "v_mfma_f32_16x16x32_bf16 v[92:95], v[24:27], v[176:179], v[92:95]\n" \
  "ds_read_b128 v[20:23], v3 offset:36864\n" \
  "v_mfma_f32_16x16x32_bf16 v[92:95], v[28:31], v[184:187], v[92:95]\n" \
  "s_add_u32 m0, s69, 114688\n" \
  "s_add_u32 s68, s68, 0x2000\n" \
  "buffer_load_dwordx4 v8, s[64:67], s68 offen sc1 lds\n" \
  "v_mfma_f32_16x16x32_bf16 v[96:99], v[24:27], v[180:183], v[96:99]\n" \
  "ds_read_b128 v[24:27], v3 offset:38912\n" \
  "v_mfma_f32_16x16x32_bf16 v[96:99], v[28:31], v[188:191], v[96:99]\n" \
  "s_waitcnt vmcnt(16)\n" \
  "s_waitcnt lgkmcnt(2)\n" \
  "v_mfma_f32_16x16x32_bf16 v[60:63], v[32:35], v[192:195], v[60:63]\n" \
  "s_waitcnt vmcnt(9)\n" \
  "s_barrier\n" \
  "ds_read_b128 v[28:31], v3 offset:40960\n" \
  "s_setprio 3\n" \
  "v_mfma_f32_16x16x32_bf16 v[60:63], v[16:19], v[200:203], v[60:63]\n" \
  "v_mfma_f32_16x16x32_bf16 v[64:67], v[32:35], v[196:199], v[64:67]\n" \
  "ds_read_b128 v[32:35], v3 offset:43008\n" \
  "v_mfma_f32_16x16x32_bf16 v[64:67], v[16:19], v[204:207], v[64:67]\n" \
  "s_add_u32 m0, s69, 122880\n" \
  "s_add_u32 s68, s68, 0x2000\n" \
  "buffer_load_dwordx4 v8, s[64:67], s68 offen sc1 lds\n" \
  "s_waitcnt lgkmcnt(2)\n" \
  "v_mfma_f32_16x16x32_bf16 v[68:71], v[20:23], v[192:195], v[68:71]\n" \
  "ds_read_b128 v[16:19], v3 offset:45056\n" \
  "v_mfma_f32_16x16x32_bf16 v[68:71], v[24:27], v[200:203], v[68:71]\n" \
  "v_mfma_f32_16x16x32_bf16 v[72:75], v[20:23], v[196:199], v[72:75]\n" \
  "ds_read_b128 v[20:23], v3 offset:47104\n" \
  "v_mfma_f32_16x16x32_bf16 v[72:75], v[24:27], v[204:207], v[72:75]\n" \
  "s_add_u32 m0, s69, 131072\n" \
  "s_add_u32 s68, s68, 0x2000\n" \
  "buffer_load_dwordx4 v8, s[64:67], s68 offen sc1 lds\n" \
  "s_waitcnt lgkmcnt(2)\n" \
  "v_mfma_f32_16x16x32_bf16 v[76:79], v[28:31], v[192:195], v[76:79]\n" \
  "ds_read_b128 v[24:27], v4 offset:0\n" \
  "s_waitcnt vmcnt(5)\n" \
  "v_cmp_gt_u32 vcc, 3, v6\n" \
  "s_cbranch_vccnz 43f\n" \
  "33:\n" \
  "v_mfma_f32_16x16x32_bf16 v[76:79], v[32:35], v[200:203], v[76:79]\n" \
  "v_mfma_f32_16x16x32_bf16 v[80:83], v[28:31], v[196:199], v[80:83]\n" \
  "ds_read_b128 v[28:31], v4 offset:2048\n" \
  "v_mfma_f32_16x16x32_bf16 v[80:83], v[32:35], v[204:207], v[80:83]\n" \
  "s_waitcnt lgkmcnt(2)\n" \
  "v_mfma_f32_16x16x32_bf16 v[84:87], v[16:19], v[192:195], v[84:87]\n" \
  "ds_read_b128 v[32:35], v4 offset:4096\n" \
  "v_mfma_f32_16x16x32_bf16 v[84:87], v[20:23], v[200:203], v[84:87]\n" \
  "s_add_u32 s76, s73, 6144\n" \
  "buffer_load_dwordx4 v[224:227], v9, s[64:67], s76 offen sc1\n" \
  "buffer_load_dwordx4 v[228:231], v10, s[64:67], s76 offen sc1\n" \
  "v_mfma_f32_16x16x32_bf16 v[88:91], v[16:19], v[196:199], v[88:91]\n" \
  "ds_read_b128 v[16:19], v4 offset:6144\n" \
  "v_mfma_f32_16x16x32_bf16 v[88:91], v[20:23], v[204:207], v[88:91]\n" \
  "s_waitcnt lgkmcnt(2)\n" \
  "v_mfma_f32_16x16x32_bf16 v[44:47], v[24:27], v[192:195], v[44:47]\n" \
  "ds_read_b128 v[20:23], v4 offset:8192\n" \
  "v_mfma_f32_16x16x32_bf16 v[44:47], v[28:31], v[200:203], v[44:47]\n" \
  "v_mfma_f32_16x16x32_bf16 v[48:51], v[24:27], v[196:199], v[48:51]\n" \
  "ds_read_b128 v[24:27], v4 offset:10240\n" \
  "s_setprio 2\n" \
  "v_mfma_f32_16x16x32_bf16 v[48:51], v[28:31], v[204:207], v[48:51]\n" \
  "s_waitcnt lgkmcnt(2)\n" \
  "v_mfma_f32_16x16x32_bf16 v[52:55], v[32:35], v[192:195], v[52:55]\n" \
  "ds_read_b128 v[28:31], v4 offset:12288\n" \
  "v_mfma_f32_16x16x32_bf16 v[52:55], v[16:19], v[200:203], v[52:55]\n" \
  "v_mfma_f32_16x16x32_bf16 v[56:59], v[32:35], v[196:199], v[56:59]\n" \
  "ds_read_b128 v[32:35], v4 offset:14336\n" \
  "v_mfma_f32_16x16x32_bf16 v[56:59], v[16:19], v[204:207], v[56:59]\n" \
  "s_add_u32 s76, s73, 16384\n" \
  "buffer_load_dwordx4 v[232:235], v9, s[64:67], s76 offen sc1\n" \
  "buffer_load_dwordx4 v[236:239], v10, s[64:67], s76 offen sc1\n" \
  "s_waitcnt lgkmcnt(2)\n" \
  "v_mfma_f32_16x16x32_bf16 v[92:95], v[20:23], v[192:195], v[92:95]\n" \
  "ds_read_b128 v[16:19], v4 offset:16384\n" \
  "v_mfma_f32_16x16x32_bf16 v[92:95], v[24:27], v[200:203], v[92:95]\n" \
  "v_mfma_f32_16x16x32_bf16 v[96:99], v[20:23], v[196:199], v[96:99]\n" \
  "ds_read_b128 v[20:23], v4 offset:18432\n" \
  "v_mfma_f32_16x16x32_bf16 v[96:99], v[24:27], v[204:207], v[96:99]\n" \
  "s_waitcnt lgkmcnt(2)\n" \
  "v_mfma_f32_16x16x32_bf16 v[60:63], v[28:31], v[208:211], v[60:63]\n" \
  "ds_read_b128 v[24:27], v4 offset:20480\n" \
  "v_mfma_f32_16x16x32_bf16 v[60:63], v[32:35], v[216:219], v[60:63]\n" \
  "v_mfma_f32_16x16x32_bf16 v[64:67], v[28:31], v[212:215], v[64:67]\n" \
  "ds_read_b128 v[28:31], v4 offset:22528\n" \
  "v_mfma_f32_16x16x32_bf16 v[64:67], v[32:35], v[220:223], v[64:67]\n" \
  "s_waitcnt lgkmcnt(2)\n" \
  "v_mfma_f32_16x16x32_bf16 v[68:71], v[16:19], v[208:211], v[68:71]\n" \
  "ds_read_b128 v[32:35], v4 offset:24576\n" \
  "s_setprio 1\n" \
  "v_mfma_f32_16x16x32_bf16 v[68:71], v[20:23], v[216:219], v[68:71]\n" \
  "s_add_u32 s76, s73, 24576\n" \
  "buffer_load_dwordx4 v[240:243], v9, s[64:67], s76 offen sc1\n" \
  "buffer_load_dwordx4 v[244:247], v10, s[64:67], s76 offen sc1\n" \
  "v_mfma_f32_16x16x32_bf16 v[72:75], v[16:19], v[212:215], v[72:75]\n" \
  "ds_read_b128 v[16:19], v4 offset:26624\n" \
  "v_mfma_f32_16x16x32_bf16 v[72:75], v[20:23], v[220:223], v[72:75]\n" \
  "s_waitcnt lgkmcnt(2)\n" \
  "v_mfma_f32_16x16x32_bf16 v[76:79], v[24:27], v[208:211], v[76:79]\n" \
  "ds_read_b128 v[20:23], v4 offset:28672\n" \
  "v_mfma_f32_16x16x32_bf16 v[76:79], v[28:31], v[216:219], v[76:79]\n" \
  "v_mfma_f32_16x16x32_bf16 v[80:83], v[24:27], v[212:215], v[80:83]\n" \
  "ds_read_b128 v[24:27], v4 offset:30720\n" \
  "s_setprio 0\n" \
  "v_mfma_f32_16x16x32_bf16 v[80:83], v[28:31], v[220:223], v[80:83]\n" \
  "s_waitcnt lgkmcnt(2)\n" \
  "v_mfma_f32_16x16x32_bf16 v[84:87], v[32:35], v[208:211], v[84:87]\n" \
  "ds_read_b128 v[28:31], v4 offset:32768\n" \
  "v_mfma_f32_16x16x32_bf16 v[84:87], v[16:19], v[216:219], v[84:87]\n" \
  "v_mfma_f32_16x16x32_bf16 v[88:91], v[32:35], v[212:215], v[88:91]\n" \
  "ds_read_b128 v[32:35], v4 offset:34816\n" \
  "v_mfma_f32_16x16x32_bf16 v[88:91], v[16:19], v[220:223], v[88:91]\n" \
  "s_add_u32 s76, s73, 34816\n" \
  "buffer_load_dwordx4 v[248:251], v9, s[64:67], s76 offen sc1\n" \
  "buffer_load_dwordx4 v[252:255], v10, s[64:67], s76 offen sc1\n" \
  "s_waitcnt lgkmcnt(2)\n" \
  "v_mfma_f32_16x16x32_bf16 v[44:47], v[20:23], v[208:211], v[44:47]\n" \
  "s_waitcnt vmcnt(9)\n" \
  "s_barrier\n" \
  "ds_read_b128 v[16:19], v4 offset:36864\n" \
  "s_setprio 3\n" \
  "v_mfma_f32_16x16x32_bf16 v[44:47], v[24:27], v[216:219], v[44:47]\n" \
  "v_mfma_f32_16x16x32_bf16 v[48:51], v[20:23], v[212:215], v[48:51]\n" \
  "ds_read_b128 v[20:23], v4 offset:38912\n" \
  "v_mfma_f32_16x16x32_bf16 v[48:51], v[24:27], v[220:223], v[48:51]\n" \
  "s_waitcnt lgkmcnt(2)\n" \
  "v_mfma_f32_16x16x32_bf16 v[52:55], v[28:31], v[208:211], v[52:55]\n" \
  "ds_read_b128 v[24:27], v4 offset:40960\n" \
  "v_mfma_f32_16x16x32_bf16 v[52:55], v[32:35], v[216:219], v[52:55]\n" \
  "v_mfma_f32_16x16x32_bf16 v[56:59], v[28:31], v[212:215], v[56:59]\n" \
  "ds_read_b128 v[28:31], v4 offset:43008\n" \
  "v_mfma_f32_16x16x32_bf16 v[56:59], v[32:35], v[220:223], v[56:59]\n" \
  "s_waitcnt lgkmcnt(2)\n" \
  "v_mfma_f32_16x16x32_bf16 v[92:95], v[16:19], v[208:211], v[92:95]\n" \
  "ds_read_b128 v[32:35], v4 offset:45056\n" \
  "v_mfma_f32_16x16x32_bf16 v[92:95], v[20:23], v[216:219], v[92:95]\n" \
  "s_add_u32 m0, s69, 139264\n" \
  "s_add_u32 s68, s68, 0x2000\n" \
  "buffer_load_dwordx4 v8, s[64:67], s68 offen sc1 lds\n" \
  "v_mfma_f32_16x16x32_bf16 v[96:99], v[16:19], v[212:215], v[96:99]\n" \
  "ds_read_b128 v[16:19], v4 offset:47104\n" \
  "v_mfma_f32_16x16x32_bf16 v[96:99], v[20:23], v[220:223], v[96:99]\n" \
  "s_waitcnt vmcnt(7)\n" \
  "s_waitcnt vmcnt(5)\n" \
  "s_waitcnt lgkmcnt(2)\n" \
  "v_mfma_f32_16x16x32_bf16 v[60:63], v[24:27], v[224:227], v[60:63]\n" \
  "ds_read_b128 v[20:23], v5 offset:0\n" \
  "v_mfma_f32_16x16x32_bf16 v[60:63], v[28:31], v[232:235], v[60:63]\n" \
  "s_add_u32 m0, s69, 0\n" \
  "s_add_u32 s68, s68, 0x2000\n" \
  "buffer_load_dwordx4 v8, s[64:67], s68 offen sc1 lds\n" \
  "v_mfma_f32_16x16x32_bf16 v[64:67], v[24:27], v[228:231], v[64:67]\n" \
  "ds_read_b128 v[24:27], v5 offset:2048\n" \
  "v_mfma_f32_16x16x32_bf16 v[64:67], v[28:31], v[236:239], v[64:67]\n" \
  "s_waitcnt lgkmcnt(2)\n" \
  "v_mfma_f32_16x16x32_bf16 v[68:71], v[32:35], v[224:227], v[68:71]\n" \
  "ds_read_b128 v[28:31], v5 offset:4096\n" \
  "v_mfma_f32_16x16x32_bf16 v[68:71], v[16:19], v[232:235], v[68:71]\n" \
  "v_mfma_f32_16x16x32_bf16 v[72:75], v[32:35], v[228:231], v[72:75]\n" \
  "ds_read_b128 v[32:35], v5 offset:6144\n" \
  "s_setprio 2\n" \
  "v_mfma_f32_16x16x32_bf16 v[72:75], v[16:19], v[236:239], v[72:75]\n" \
  "s_add_u32 m0, s69, 8192\n" \
  "s_add_u32 s68, s68, 0x2000\n" \
  "buffer_load_dwordx4 v8, s[64:67], s68 offen sc1 lds\n" \
  "s_waitcnt lgkmcnt(2)\n" \
  "v_mfma_f32_16x16x32_bf16 v[76:79], v[20:23], v[224:227], v[76:79]\n" \
  "ds_read_b128 v[16:19], v5 offset:8192\n" \
  "v_mfma_f32_16x16x32_bf16 v[76:79], v[24:27], v[232:235], v[76:79]\n" \
  "v_mfma_f32_16x16x32_bf16 v[80:83], v[20:23], v[228:231], v[80:83]\n" \
  "ds_read_b128 v[20:23], v5 offset:10240\n" \
  "v_mfma_f32_16x16x32_bf16 v[80:83], v[24:27], v[236:239], v[80:83]\n" \
  "s_add_u32 m0, s69, 16384\n" \
  "s_add_u32 s68, s68, 0x2000\n" \
  "buffer_load_dwordx4 v8, s[64:67], s68 offen sc1 lds\n" \
  "s_waitcnt lgkmcnt(2)\n" \
  "v_mfma_f32_16x16x32_bf16 v[84:87], v[28:31], v[224:227], v[84:87]\n" \
  "ds_read_b128 v[24:27], v5 offset:12288\n" \
  "v_mfma_f32_16x16x32_bf16 v[84:87], v[32:35], v[232:235], v[84:87]\n" \
  "v_mfma_f32_16x16x32_bf16 v[88:91], v[28:31], v[228:231], v[88:91]\n" \
  "ds_read_b128 v[28:31], v5 offset:14336\n" \
  "v_mfma_f32_16x16x32_bf16 v[88:91], v[32:35], v[236:239], v[88:91]\n" \
  "s_waitcnt lgkmcnt(2)\n" \
  "v_mfma_f32_16x16x32_bf16 v[44:47], v[16:19], v[224:227], v[44:47]\n" \
  "ds_read_b128 v[32:35], v5 offset:16384\n" \
  "v_mfma_f32_16x16x32_bf16 v[44:47], v[20:23], v[232:235], v[44:47]\n" \
  "s_add_u32 m0, s69, 24576\n" \
  "s_add_u32 s68, s68, 0x2000\n" \
  "buffer_load_dwordx4 v8, s[64:67], s68 offen sc1 lds\n" \
  "v_mfma_f32_16x16x32_bf16 v[48:51], v[16:19], v[228:231], v[48:51]\n" \
  "ds_read_b128 v[16:19], v5 offset:18432\n" \
  "s_setprio 1\n" \
  "v_mfma_f32_16x16x32_bf16 v[48:51], v[20:23], v[236:239], v[48:51]\n" \
  "s_waitcnt lgkmcnt(2)\n" \
  "v_mfma_f32_16x16x32_bf16 v[52:55], v[24:27], v[224:227], v[52:55]\n" \
  "ds_read_b128 v[20:23], v5 offset:20480\n" \
  "v_mfma_f32_16x16x32_bf16 v[52:55], v[28:31], v[232:235], v[52:55]\n" \
  "s_add_u32 m0, s69, 32768\n" \
  "s_add_u32 s68, s68, 0x2000\n" \
  "buffer_load_dwordx4 v8, s[64:67], s68 offen sc1 lds\n" \
  "v_mfma_f32_16x16x32_bf16 v[56:59], v[24:27], v[228:231], v[56:59]\n" \
  "ds_read_b128 v[24:27], v5 offset:22528\n" \
  "v_mfma_f32_16x16x32_bf16 v[56:59], v[28:31], v[236:239], v[56:59]\n" \
  "s_waitcnt lgkmcnt(2)\n" \
  "v_mfma_f32_16x16x32_bf16 v[92:95], v[32:35], v[224:227], v[92:95]\n" \
  "ds_read_b128 v[28:31], v5 offset:24576\n" \
  "s_setprio 0\n" \
  "v_mfma_f32_16x16x32_bf16 v[92:95], v[16:19], v[232:235], v[92:95]\n" \
  "v_mfma_f32_16x16x32_bf16 v[96:99], v[32:35], v[228:231], v[96:99]\n" \
  "ds_read_b128 v[32:35], v5 offset:26624\n" \
  "v_mfma_f32_16x16x32_bf16 v[96:99], v[16:19], v[236:239], v[96:99]\n" \
  "s_add_u32 m0, s69, 40960\n" \
  "s_add_u32 s68, s68, 0x2000\n" \
  "buffer_load_dwordx4 v8, s[64:67], s68 offen sc1 lds\n" \
  "s_waitcnt vmcnt(9)\n" \
  "s_waitcnt vmcnt(7)\n" \
  "s_waitcnt lgkmcnt(2)\n" \
  "v_mfma_f32_16x16x32_bf16 v[60:63], v[20:23], v[240:243], v[60:63]\n" \
  "s_waitcnt vmcnt(4)\n" \
  "s_barrier\n" \
  "ds_read_b128 v[16:19], v5 offset:28672\n" \
  "s_setprio 3\n" \
  "v_mfma_f32_16x16x32_bf16 v[60:63], v[24:27], v[248:251], v[60:63]\n" \
  "v_mfma_f32_16x16x32_bf16 v[64:67], v[20:23], v[244:247], v[64:67]\n" \
  "ds_read_b128 v[20:23], v5 offset:30720\n" \
  "v_mfma_f32_16x16x32_bf16 v[64:67], v[24:27], v[252:255], v[64:67]\n" \
  "s_add_u32 m0, s69, 49152\n" \
  "s_add_u32 s68, s68, 0x2000\n" \
  "buffer_load_dwordx4 v8, s[64:67], s68 offen sc1 lds\n" \
  "s_waitcnt lgkmcnt(2)\n" \
  "v_mfma_f32_16x16x32_bf16 v[68:71], v[28:31], v[240:243], v[68:71]\n" \
  "ds_read_b128 v[24:27], v5 offset:32768\n" \
  "v_mfma_f32_16x16x32_bf16 v[68:71], v[32:35], v[248:251], v[68:71]\n" \
  "v_mfma_f32_16x16x32_bf16 v[72:75], v[28:31], v[244:247], v[72:75]\n" \
  "ds_read_b128 v[28:31], v5 offset:34816\n" \
  "v_mfma_f32_16x16x32_bf16 v[72:75], v[32:35], v[252:255], v[72:75]\n" \
  "s_waitcnt lgkmcnt(2)\n" \
  "v_mfma_f32_16x16x32_bf16 v[76:79], v[16:19], v[240:243], v[76:79]\n" \
  "ds_read_b128 v[32:35], v5 offset:36864\n" \
  "v_mfma_f32_16x16x32_bf16 v[76:79], v[20:23], v[248:251], v[76:79]\n" \
  "ds_write_b128 v13, v[64:67] offset:0\n" \
  "s_add_u32 m0, s69, 57344\n" \
  "s_add_u32 s68, s68, 0x2000\n" \
  "buffer_load_dwordx4 v8, s[64:67], s68 offen sc1 lds\n" \
  "v_mfma_f32_16x16x32_bf16 v[80:83], v[16:19], v[244:247], v[80:83]\n" \
  "ds_read_b128 v[16:19], v5 offset:38912\n" \
  "v_mfma_f32_16x16x32_bf16 v[80:83], v[20:23], v[252:255], v[80:83]\n" \
  "s_waitcnt lgkmcnt(3)\n" \
  "v_mfma_f32_16x16x32_bf16 v[84:87], v[24:27], v[240:243], v[84:87]\n" \
  "ds_read_b128 v[20:23], v5 offset:40960\n" \
  "v_mfma_f32_16x16x32_bf16 v[84:87], v[28:31], v[248:251], v[84:87]\n" \
  "ds_write_b128 v13, v[72:75] offset:1024\n" \
  "s_add_u32 m0, s69, 65536\n" \
  "s_add_u32 s68, s68, 0x2000\n" \
  "buffer_load_dwordx4 v8, s[64:67], s68 offen sc1 lds\n" \
  "v_mfma_f32_16x16x32_bf16 v[88:91], v[24:27], v[244:247], v[88:91]\n" \
  "ds_read_b128 v[24:27], v5 offset:43008\n" \
  "v_mfma_f32_16x16x32_bf16 v[88:91], v[28:31], v[252:255], v[88:91]\n" \
  "s_waitcnt lgkmcnt(3)\n" \
  "v_mfma_f32_16x16x32_bf16 v[44:47], v[32:35], v[240:243], v[44:47]\n" \
  "ds_read_b128 v[28:31], v5 offset:45056\n" \
  "s_setprio 2\n" \
  "v_mfma_f32_16x16x32_bf16 v[44:47], v[16:19], v[248:251], v[44:47]\n" \
  "ds_write_b128 v13, v[80:83] offset:2048\n" \
  "v_mfma_f32_16x16x32_bf16 v[48:51], v[32:35], v[244:247], v[48:51]\n" \
  "ds_read_b128 v[32:35], v5 offset:47104\n" \
  "v_mfma_f32_16x16x32_bf16 v[48:51], v[16:19], v[252:255], v[48:51]\n" \
  "s_waitcnt lgkmcnt(3)\n" \
  "v_mfma_f32_16x16x32_bf16 v[52:55], v[20:23], v[240:243], v[52:55]\n" \
  "ds_read_b128 v[16:19], v3 offset:0\n" \
  "v_mfma_f32_16x16x32_bf16 v[52:55], v[24:27], v[248:251], v[52:55]\n" \
  "ds_write_b128 v13, v[88:91] offset:3072\n" \
  "v_mfma_f32_16x16x32_bf16 v[56:59], v[20:23], v[244:247], v[56:59]\n" \
  "ds_read_b128 v[20:23], v3 offset:2048\n" \
  "v_mfma_f32_16x16x32_bf16 v[56:59], v[24:27], v[252:255], v[56:59]\n" \
  "s_waitcnt lgkmcnt(3)\n" \
  "v_mfma_f32_16x16x32_bf16 v[92:95], v[28:31], v[240:243], v[92:95]\n" \
  "ds_read_b128 v[24:27], v3 offset:4096\n" \
  "v_mfma_f32_16x16x32_bf16 v[92:95], v[32:35], v[248:251], v[92:95]\n" \
  "ds_write_b128 v13, v[48:51] offset:4096\n" \
  "v_mfma_f32_16x16x32_bf16 v[96:99], v[28:31], v[244:247], v[96:99]\n" \
  "ds_read_b128 v[28:31], v3 offset:6144\n" \
  "s_setprio 1\n" \
  "v_mfma_f32_16x16x32_bf16 v[96:99], v[32:35], v[252:255], v[96:99]\n" \
  "s_waitcnt lgkmcnt(3)\n" \
  "v_mfma_f32_16x16x32_bf16 v[100:103], v[16:19], v[112:115], 0\n" \
  "ds_read_b128 v[32:35], v3 offset:8192\n" \
  "v_mfma_f32_16x16x32_bf16 v[100:103], v[20:23], v[120:123], v[100:103]\n" \
  "ds_write_b128 v13, v[56:59] offset:5120\n" \
  "v_mfma_f32_16x16x32_bf16 v[108:111], v[16:19], v[116:119], 0\n" \
  "ds_read_b128 v[16:19], v3 offset:10240\n" \
  "v_mfma_f32_16x16x32_bf16 v[108:111], v[20:23], v[124:127], v[108:111]\n" \
  "s_waitcnt lgkmcnt(3)\n" \
  "v_mfma_f32_16x16x32_bf16 v[104:107], v[24:27], v[112:115], 0\n" \
  "ds_read_b128 v[20:23], v3 offset:12288\n" \
  "s_setprio 0\n" \
  "v_mfma_f32_16x16x32_bf16 v[104:107], v[28:31], v[120:123], v[104:107]\n" \
  "ds_write_b128 v13, v[96:99] offset:6144\n" \
  "v_mfma_f32_16x16x32_bf16 v[36:39], v[24:27], v[116:119], 0\n" \
  "ds_read_b128 v[24:27], v3 offset:14336\n" \
  "v_mfma_f32_16x16x32_bf16 v[36:39], v[28:31], v[124:127], v[36:39]\n" \
  "s_waitcnt lgkmcnt(3)\n" \
  "v_mfma_f32_16x16x32_bf16 v[100:103], v[32:35], v[128:131], v[100:103]\n" \
  "s_waitcnt vmcnt(0)\n" \
  "s_waitcnt lgkmcnt(1)\n" \
  "s_barrier\n" \
  "ds_read_b128 v[28:31], v3 offset:16384\n" \
  "s_setprio 3\n" \
  "ds_read_b128 v[64:67], v14 offset:0\n" \
  "v_mfma_f32_16x16x32_bf16 v[100:103], v[16:19], v[136:139], v[100:103]\n" \
  "ds_read_b128 v[72:75], v14 offset:1024\n" \
  "buffer_load_dwordx2 v[112:113], v11, s[64:67], s82 offen offset:0 sc1\n" \
  "v_mfma_f32_16x16x32_bf16 v[108:111], v[32:35], v[132:135], v[108:111]\n" \
  "ds_read_b128 v[32:35], v3 offset:18432\n" \
  "ds_read_b128 v[80:83], v14 offset:2048\n" \
  "v_mfma_f32_16x16x32_bf16 v[108:111], v[16:19], v[140:143], v[108:111]\n" \
  "ds_read_b128 v[88:91], v14 offset:3072\n" \
  "s_waitcnt lgkmcnt(6)\n" \
  "v_mfma_f32_16x16x32_bf16 v[104:107], v[20:23], v[128:131], v[104:107]\n" \
  "ds_read_b128 v[16:19], v3 offset:20480\n" \
  "ds_read_b128 v[48:51], v14 offset:4096\n" \
  "v_lshrrev_b32 v7, 1, v8\n" \
  "v_add_u32 v7, s88, v7\n" \
  "s_waitcnt lgkmcnt(6)\n" \
  "v_add_f32 v60, v60, v64\n" \
  "v_add_f32 v61, v61, v65\n" \
  "v_mfma_f32_16x16x32_bf16 v[104:107], v[24:27], v[136:139], v[104:107]\n" \
  "ds_read_b128 v[56:59], v14 offset:5120\n" \
  "v_add_f32 v62, v62, v66\n" \
  "v_add_f32 v63, v63, v67\n" \
  "v_cvt_pk_bf16_f32 v64, v60, v61\n" \
  "v_cvt_pk_bf16_f32 v65, v62, v63\n" \
  "v_min_u32 v64, -2, v64\n" \
  "buffer_load_dwordx2 v[114:115], v11, s[64:67], s82 offen offset:1024 sc1\n" \
  "v_mfma_f32_16x16x32_bf16 v[36:39], v[20:23], v[132:135], v[36:39]\n" \
  "ds_read_b128 v[20:23], v3 offset:22528\n" \
  "ds_read_b128 v[96:99], v14 offset:6144\n" \
  "v_min_u32 v65, -2, v65\n" \
  "s_waitcnt lgkmcnt(8)\n" \
  "v_add_f32 v68, v68, v72\n" \
  "v_add_f32 v69, v69, v73\n" \
  "v_add_f32 v70, v70, v74\n" \
  "v_mfma_f32_16x16x32_bf16 v[36:39], v[24:27], v[140:143], v[36:39]\n" \
  "v_add_f32 v71, v71, v75\n" \
  "v_cvt_pk_bf16_f32 v66, v68, v69\n" \
  "v_cvt_pk_bf16_f32 v67, v70, v71\n" \
  "v_min_u32 v66, -2, v66\n" \
  "v_min_u32 v67, -2, v67\n" \
  "s_waitcnt lgkmcnt(7)\n" \
  "v_mfma_f32_16x16x32_bf16 v[100:103], v[28:31], v[144:147], v[100:103]\n" \
  "ds_read_b128 v[24:27], v3 offset:24576\n" \
  "buffer_store_dwordx4 v[64:67], v8, s[64:67], s85 offen sc1\n" \
  "s_waitcnt lgkmcnt(7)\n" \
  "v_add_f32 v76, v76, v80\n" \
  "v_add_f32 v77, v77, v81\n" \
  "v_add_f32 v78, v78, v82\n" \
  "v_mfma_f32_16x16x32_bf16 v[100:103], v[32:35], v[152:155], v[100:103]\n" \
  "v_add_f32 v79, v79, v83\n" \
  "v_cvt_pk_bf16_f32 v80, v76, v77\n" \
  "v_cvt_pk_bf16_f32 v81, v78, v79\n" \
  "v_min_u32 v80, -2, v80\n" \
  "v_min_u32 v81, -2, v81\n" \
  "buffer_load_dwordx2 v[116:117], v11, s[64:67], s83 offen sc1\n" \
  "v_mfma_f32_16x16x32_bf16 v[108:111], v[28:31], v[148:151], v[108:111]\n" \
  "ds_read_b128 v[28:31], v3 offset:26624\n" \
  "s_waitcnt lgkmcnt(7)\n" \
  "v_add_f32 v84, v84, v88\n" \
  "v_add_f32 v85, v85, v89\n" \
  "v_add_f32 v86, v86, v90\n" \
  "v_add_f32 v87, v87, v91\n" \
  "v_mfma_f32_16x16x32_bf16 v[108:111], v[32:35], v[156:159], v[108:111]\n" \
  "v_cvt_pk_bf16_f32 v82, v84, v85\n" \
  "v_cvt_pk_bf16_f32 v83, v86, v87\n" \
  "v_min_u32 v82, -2, v82\n" \
  "v_min_u32 v83, -2, v83\n" \
  "buffer_store_dwordx4 v[80:83], v8, s[64:67], s86 offen sc1\n" \
  "s_waitcnt lgkmcnt(3)\n" \
  "v_mfma_f32_16x16x32_bf16 v[104:107], v[16:19], v[144:147], v[104:107]\n" \
  "ds_read_b128 v[32:35], v3 offset:28672\n" \
  "v_add_f32 v44, v44, v48\n" \
  "v_add_f32 v45, v45, v49\n" \
  "v_add_f32 v46, v46, v50\n" \
  "v_add_f32 v47, v47, v51\n" \
  "v_mfma_f32_16x16x32_bf16 v[104:107], v[20:23], v[152:155], v[104:107]\n" \
  "v_cvt_pk_bf16_f32 v48, v44, v45\n" \
  "v_cvt_pk_bf16_f32 v49, v46, v47\n" \
  "v_min_u32 v48, -2, v48\n" \
  "v_min_u32 v49, -2, v49\n" \
  "v_mfma_f32_16x16x32_bf16 v[36:39], v[16:19], v[148:151], v[36:39]\n" \
  "ds_read_b128 v[16:19], v3 offset:30720\n" \
  "v_add_f32 v52, v52, v56\n" \
  "v_add_f32 v53, v53, v57\n" \
  "v_add_f32 v54, v54, v58\n" \
  "v_add_f32 v55, v55, v59\n" \
  "v_cvt_pk_bf16_f32 v50, v52, v53\n" \
  "v_mfma_f32_16x16x32_bf16 v[36:39], v[20:23], v[156:159], v[36:39]\n" \
  "v_cvt_pk_bf16_f32 v51, v54, v55\n" \
  "v_min_u32 v50, -2, v50\n" \
  "v_min_u32 v51, -2, v51\n" \
  "buffer_store_dwordx4 v[48:51], v8, s[64:67], s87 offen sc1\n" \
  "s_waitcnt lgkmcnt(4)\n" \
  "s_waitcnt lgkmcnt(2)\n" \
  "v_mfma_f32_16x16x32_bf16 v[100:103], v[24:27], v[160:163], v[100:103]\n" \
  "ds_read_b128 v[20:23], v3 offset:32768\n" \
  "v_add_f32 v92, v92, v96\n" \
  "v_add_f32 v93, v93, v97\n" \
  "v_add_f32 v94, v94, v98\n" \
  "v_add_f32 v95, v95, v99\n" \
  "v_cvt_pk_bf16_f32 v96, v92, v93\n" \
  "v_mfma_f32_16x16x32_bf16 v[100:103], v[28:31], v[168:171], v[100:103]\n" \
  "v_cvt_pk_bf16_f32 v97, v94, v95\n" \
  "v_min_u32 v96, -2, v96\n" \
  "v_min_u32 v97, -2, v97\n" \
  "buffer_store_dwordx2 v[96:97], v7, s[64:67], 0 offen sc1\n" \
  "v_mfma_f32_16x16x32_bf16 v[108:111], v[24:27], v[164:167], v[108:111]\n" \
  "ds_read_b128 v[24:27], v3 offset:34816\n" \
  "v_mfma_f32_16x16x32_bf16 v[108:111], v[28:31], v[172:175], v[108:111]\n" \
  "s_waitcnt lgkmcnt(2)\n" \
  "v_mfma_f32_16x16x32_bf16 v[104:107], v[32:35], v[160:163], v[104:107]\n" \
  "ds_read_b128 v[28:31], v3 offset:36864\n" \
  "v_mfma_f32_16x16x32_bf16 v[104:107], v[16:19], v[168:171], v[104:107]\n" \
  "v_mfma_f32_16x16x32_bf16 v[36:39], v[32:35], v[164:167], v[36:39]\n" \
  "ds_read_b128 v[32:35], v3 offset:38912\n" \
  "v_mfma_f32_16x16x32_bf16 v[36:39], v[16:19], v[172:175], v[36:39]\n" \
  "s_waitcnt lgkmcnt(2)\n" \
  "v_mfma_f32_16x16x32_bf16 v[100:103], v[20:23], v[176:179], v[100:103]\n" \
  "ds_read_b128 v[16:19], v3 offset:40960\n" \
  "v_mfma_f32_16x16x32_bf16 v[100:103], v[24:27], v[184:187], v[100:103]\n" \
  "v_mfma_f32_16x16x32_bf16 v[108:111], v[20:23], v[180:183], v[108:111]\n" \
  "ds_read_b128 v[20:23], v3 offset:43008\n" \
  "v_mfma_f32_16x16x32_bf16 v[108:111], v[24:27], v[188:191], v[108:111]\n" \
  "s_waitcnt lgkmcnt(2)\n" \
  "v_mfma_f32_16x16x32_bf16 v[104:107], v[28:31], v[176:179], v[104:107]\n" \
  "ds_read_b128 v[24:27], v3 offset:45056\n" \
  "s_setprio 2\n" \
  "v_mfma_f32_16x16x32_bf16 v[104:107], v[32:35], v[184:187], v[104:107]\n" \
  "v_mfma_f32_16x16x32_bf16 v[36:39], v[28:31], v[180:183], v[36:39]\n" \
  "ds_read_b128 v[28:31], v3 offset:47104\n" \
  "v_mfma_f32_16x16x32_bf16 v[36:39], v[32:35], v[188:191], v[36:39]\n" \
  "s_waitcnt lgkmcnt(2)\n" \
  "v_mfma_f32_16x16x32_bf16 v[100:103], v[16:19], v[192:195], v[100:103]\n" \
  "ds_read_b128 v[32:35], v4 offset:0\n" \
  "v_mfma_f32_16x16x32_bf16 v[100:103], v[20:23], v[200:203], v[100:103]\n" \
  "v_mfma_f32_16x16x32_bf16 v[108:111], v[16:19], v[196:199], v[108:111]\n" \
  "ds_read_b128 v[16:19], v4 offset:2048\n" \
  "v_mfma_f32_16x16x32_bf16 v[108:111], v[20:23], v[204:207], v[108:111]\n" \
  "s_waitcnt lgkmcnt(2)\n" \
  "v_mfma_f32_16x16x32_bf16 v[104:107], v[24:27], v[192:195], v[104:107]\n" \
  "ds_read_b128 v[20:23], v4 offset:4096\n" \
  "v_mfma_f32_16x16x32_bf16 v[104:107], v[28:31], v[200:203], v[104:107]\n" \
  "v_mfma_f32_16x16x32_bf16 v[36:39], v[24:27], v[196:199], v[36:39]\n" \
  "ds_read_b128 v[24:27], v4 offset:6144\n" \
  "v_mfma_f32_16x16x32_bf16 v[36:39], v[28:31], v[204:207], v[36:39]\n" \
  "s_waitcnt lgkmcnt(2)\n" \
  "v_mfma_f32_16x16x32_bf16 v[100:103], v[32:35], v[208:211], v[100:103]\n" \
  "ds_read_b128 v[28:31], v4 offset:8192\n" \
  "v_mfma_f32_16x16x32_bf16 v[100:103], v[16:19], v[216:219], v[100:103]\n" \
  "v_mfma_f32_16x16x32_bf16 v[108:111], v[32:35], v[212:215], v[108:111]\n" \
  "ds_read_b128 v[32:35], v4 offset:10240\n" \
  "v_mfma_f32_16x16x32_bf16 v[108:111], v[16:19], v[220:223], v[108:111]\n" \
  "s_waitcnt lgkmcnt(2)\n" \
  "v_mfma_f32_16x16x32_bf16 v[104:107], v[20:23], v[208:211], v[104:107]\n" \
  "ds_read_b128 v[16:19], v4 offset:12288\n" \
  "v_mfma_f32_16x16x32_bf16 v[104:107], v[24:27], v[216:219], v[104:107]\n" \
  "v_mfma_f32_16x16x32_bf16 v[36:39], v[20:23], v[212:215], v[36:39]\n" \
  "ds_read_b128 v[20:23], v4 offset:14336\n" \
  "v_mfma_f32_16x16x32_bf16 v[36:39], v[24:27], v[220:223], v[36:39]\n" \
  "s_waitcnt lgkmcnt(2)\n" \
  "v_mfma_f32_16x16x32_bf16 v[100:103], v[28:31], v[224:227], v[100:103]\n" \
  "ds_read_b128 v[24:27], v4 offset:16384\n" \
  "s_setprio 1\n" \
  "v_mfma_f32_16x16x32_bf16 v[100:103], v[32:35], v[232:235], v[100:103]\n" \
  "v_mfma_f32_16x16x32_bf16 v[108:111], v[28:31], v[228:231], v[108:111]\n" \
  "ds_read_b128 v[28:31], v4 offset:18432\n" \
  "v_mfma_f32_16x16x32_bf16 v[108:111], v[32:35], v[236:239], v[108:111]\n" \
  "s_waitcnt lgkmcnt(2)\n" \
  "v_mfma_f32_16x16x32_bf16 v[104:107], v[16:19], v[224:227], v[104:107]\n" \
  "ds_read_b128 v[32:35], v4 offset:20480\n" \
  "v_mfma_f32_16x16x32_bf16 v[104:107], v[20:23], v[232:235], v[104:107]\n" \
  "v_mfma_f32_16x16x32_bf16 v[36:39], v[16:19], v[228:231], v[36:39]\n" \
  "ds_read_b128 v[16:19], v4 offset:22528\n" \
  "v_mfma_f32_16x16x32_bf16 v[36:39], v[20:23], v[236:239], v[36:39]\n" \
  "s_waitcnt lgkmcnt(2)\n" \
  "v_mfma_f32_16x16x32_bf16 v[100:103], v[24:27], v[240:243], v[100:103]\n" \
  "s_setprio 0\n" \
  "v_mfma_f32_16x16x32_bf16 v[100:103], v[28:31], v[248:251], v[100:103]\n" \
  "v_mfma_f32_16x16x32_bf16 v[108:111], v[24:27], v[244:247], v[108:111]\n" \
  "v_mfma_f32_16x16x32_bf16 v[108:111], v[28:31], v[252:255], v[108:111]\n" \
  "s_waitcnt lgkmcnt(0)\n" \
  "v_mfma_f32_16x16x32_bf16 v[104:107], v[32:35], v[240:243], v[104:107]\n" \
  "v_mfma_f32_16x16x32_bf16 v[104:107], v[16:19], v[248:251], v[104:107]\n" \
  "v_mfma_f32_16x16x32_bf16 v[36:39], v[32:35], v[244:247], v[36:39]\n" \
  "v_mfma_f32_16x16x32_bf16 v[36:39], v[16:19], v[252:255], v[36:39]\n" \
  "s_setprio 0\n" \
  "v_lshrrev_b32 v7, 1, v8\n" \
  "v_add_u32 v7, s90, v7\n" \
  "s_waitcnt vmcnt(3)\n" \
  "ds_write_b128 v14, v[108:111]\n" \
  "s_nop 3\n" \
  "ds_write_b128 v14, v[36:39] offset:1024\n" \
  "buffer_load_dwordx4 v[120:123], v8, s[64:67], s89 offen offset:0 sc1\n" \
  "buffer_load_dwordx4 v[124:127], v8, s[64:67], s89 offen offset:1024 sc1\n" \
  "buffer_load_dwordx4 v[128:131], v8, s[64:67], s89 offen offset:2048 sc1\n" \
  "buffer_load_dwordx2 v[132:133], v7, s[64:67], 0 offen offset:0 sc1\n" \
  "buffer_load_dwordx2 v[134:135], v7, s[64:67], 0 offen offset:512 sc1\n" \
  "buffer_load_dwordx2 v[136:137], v7, s[64:67], 0 offen offset:1024 sc1\n" \
  "s_waitcnt lgkmcnt(0)\n" \
  "s_barrier\n" \
  "ds_read_b128 v[108:111], v13 offset:0\n" \
  "ds_read_b128 v[36:39], v13 offset:1024\n" \
  "s_waitcnt lgkmcnt(1)\n" \
  "v_pk_add_f32 v[100:101], v[100:101], v[108:109]\n" \
  "v_pk_add_f32 v[102:103], v[102:103], v[110:111]\n" \
  "s_waitcnt lgkmcnt(0)\n" \
  "v_pk_add_f32 v[104:105], v[104:105], v[36:37]\n" \
  "v_pk_add_f32 v[106:107], v[106:107], v[38:39]\n" \
  "s_branch 60f\n" \
  "48:\n" \
  "s_mov_b32 s78, 0x40000\n" \
  "58:\n" \
  "s_sleep 1\n" \
  "buffer_load_dword v6, v16, s[64:67], 0 offen sc1\n" \
  "s_sub_u32 s78, s78, 1\n" \
  "s_cbranch_scc1 38b\n" \
  "s_waitcnt vmcnt(0)\n" \
  "v_cmp_eq_u32 vcc, 0, v6\n" \
  "s_cbranch_vccnz 58b\n" \
  "s_branch 38b\n" \
  "49:\n" \
  "s_mov_b32 s78, 0x40000\n" \
  "59:\n" \
  "s_sleep 1\n" \
  "buffer_load_dword v6, v18, s[64:67], 0 offen sc1\n" \
  "s_sub_u32 s78, s78, 1\n" \
  "s_cbranch_scc1 39b\n" \
  "s_waitcnt vmcnt(0)\n" \
  "v_cmp_eq_u32 vcc, 0, v6\n" \
  "s_cbranch_vccnz 59b\n" \
  "s_branch 39b\n" \
  "41:\n" \
  "s_mov_b32 s78, 0x40000\n" \
  "51:\n" \
  "s_sleep 1\n" \
  "buffer_load_dword v6, v12, s[64:67], 0 offen sc1\n" \
  "s_sub_u32 s78, s78, 1\n" \
  "s_cbranch_scc1 31b\n" \
  "s_waitcnt vmcnt(0)\n" \
  "v_cmp_gt_u32 vcc, 1, v6\n" \
  "s_cbranch_vccnz 51b\n" \
  "s_branch 31b\n" \
  "42:\n" \
  "s_mov_b32 s78, 0x40000\n" \
  "52:\n" \
  "s_sleep 1\n" \
  "buffer_load_dword v6, v12, s[64:67], 0 offen sc1\n" \
  "s_sub_u32 s78, s78, 1\n" \
  "s_cbranch_scc1 32b\n" \
  "s_waitcnt vmcnt(0)\n" \
  "v_cmp_gt_u32 vcc, 2, v6\n" \
  "s_cbranch_vccnz 52b\n" \
  "s_branch 32b\n" \
  "43:\n" \
  "s_mov_b32 s78, 0x40000\n" \
  "53:\n" \
  "s_sleep 1\n" \
  "buffer_load_dword v6, v12, s[64:67], 0 offen sc1\n" \
  "s_sub_u32 s78, s78, 1\n" \
  "s_cbranch_scc1 33b\n" \
  "s_waitcnt vmcnt(0)\n" \
  "v_cmp_gt_u32 vcc, 3, v6\n" \
  "s_cbranch_vccnz 53b\n" \
  "s_branch 33b\n" \
  "60:\n"
#define FFN_CLOB "v3","v4","v5","v7","v20","v21","v22","v23","v24","v25","v26","v27","v28","v29","v30","v31","v32","v33","v34","v35","v36","v37","v38","v39","v40","v41","v42","v43","v44","v45","v46","v47","v48","v49","v50","v51","v52","v53","v54","v55","v56","v57","v58","v59","v60","v61","v62","v63","v64","v65","v66","v67","v68","v69","v70","v71","v72","v73","v74","v75","v76","v77","v78","v79","v80","v81","v82","v83","v84","v85","v86","v87","v88","v89","v90","v91","v96","v97","v98","v99","v108","v109","v110","v111","v144","v145","v146","v147","v148","v149","v150","v151","v152","v153","v154","v155","v156","v157","v158","v159","v160","v161","v162","v163","v164","v165","v166","v167","v168","v169","v170","v171","v172","v173","v174","v175","v176","v177","v178","v179","v180","v181","v182","v183","v184","v185","v186","v187","v188","v189","v190","v191","v192","v193","v194","v195","v196","v197","v198","v199","v200","v201","v202","v203","v204","v205","v206","v207","v208","v209","v210","v211","v212","v213","v214","v215","v216","v217","v218","v219","v220","v221","v222","v223","v224","v225","v226","v227","v228","v229","v230","v231","v232","v233","v234","v235","v236","v237","v238","v239","v240","v241","v242","v243","v244","v245","v246","v247","v248","v249","v250","v251","v252","v253","v254","v255"

DEVI void retile_plan(int job, int g4, const char* wg, const char* wu, const char* wd, u32x4& rs, u32x4& dst) {
  const int mat = job / 162, rem = job - mat * 162, ksl = rem / 9, cbl = rem - 9 * ksl, pitch = mat < 2 ? 9216 : 1152;
  const int cb = mat < 2 ? g4 * 9 + cbl : cbl, ks = mat < 2 ? ksl : g4 * 18 + ksl;
  const size_t src = (size_t)(mat == 0 ? wg : mat == 1 ? wu : wd) + ks * 32 * pitch + cb * 128;
  rs = u32x4{(unsigned)src, (unsigned)(src >> 32) & 0xffffu, mat < 3 ? 31u * pitch + 128u : 0u, 0u};
  const int pi = ksl / 9, pp = ksl - 9 * pi, ps = pp < 4 ? pp : pp == 4 ? 0 : pp - 5;
  const int slot = ps == 0 ? pi * 3 + (pp == 0 ? 0 : pp - 3) : 2 + ps * 4 + pi * 2 + (pp > 4), k4 = g4 >> 1;
#pragma unroll
  for (int i = 0; i < 4; ++i) {
    const int half = i & 1, b32 = 2 * cb + (i >> 1), m = b32 / 9, pos = b32 - 9 * m, grp = 2 * m + (pos == 4 ? half : pos > 4), pr = pos < 4 ? pos : pos - 5;
    const int pg = pos == 4 ? ks * 6 + 4 + mat : pr == 0 ? ks * 6 + half + 2 * mat : pr == 3 ? 252 + ks * 4 + half + 2 * mat : 36 + pr * 72 + half * 36 + mat * 18 + ks;
    const int lq = pos == 4 ? 8 : 2 * ((pr - k4) & 3) + half, pd = 2 * ((lq < 2 ? 126 + (slot >> 1) * 4 + lq * 2 : (slot >> 1) * 14 + (lq - 2) * 2) + (slot & 1)) + (g4 & 1);
    dst[i] = mat < 2 ? (unsigned)WS_WGU + (grp * 324 + pg) * 1024 : mat == 2 ? (unsigned)WS_WD + ((k4 * 4 + grp) * 324 + pd) * 1024 : 0x7f000000u;
  }
}
DEVI unsigned load_tag(const char* ws) {
  unsigned t; asm volatile("s_load_dword %0, %1, %2\n\ts_waitcnt lgkmcnt(0)\n\ts_add_u32 %0, %0, 1" : "=&s"(t) : "s"(ws), "s"((unsigned)WS_EP) : "memory", "scc"); return t; }
DEVI void phase_qkv(const char* X, const char* W, const char* WO, const char* WG, const char* WU, const char* WD, char* ws, LAS char* lds, int wid, unsigned& tag) {
  const int lane = lane_id(), tid = wid * 64 + lane, xcd = blockIdx.x & 7, j = blockIdx.x >> 3;
  const int l15 = lane & 15, g = lane >> 4;
  if (j >= 28) {
    const int sp = (j - 28) * 8 + xcd, ks = 31 - sp;
#define PFS 5
    const int job = sp < 14 ? sp * 8 + wid : wid < 3 ? 70 + sp * 3 + wid : 162, wk = job / 9, cb = job - wk * 9;
    if (job < 162) { const i32x4 rs = mkrsrc(WO, 576 * 1152);
#pragma unroll
      for (int q = 0; q < 4; ++q) dma(rs, wo_row(wk, q, lane >> 3) * 1152 + (lane & 7) * 16, cb * 128, wid * 4096 + q * 1024); }
    if (ks < 18) { const i32x4 rs = mkrsrc(WO, 576 * 1152);
#pragma unroll
      for (int q = 0; q < 5; ++q) { const int c = min(q * 64 + lane, 287), rr = (c * 911) >> 16, slot = wid * 4 + rr;
        dma(rs, wo_row(ks, slot >> 3, slot & 7) * 1152 + (c - 72 * rr) * 16, 0, 36864 + wid * 5120 + q * 1024); } }
    { const int w32 = (j - 28) * 8 + wid, n = w32 * 44 + (lane < 44 ? lane : 0), m = n - 512, wr = (m * 4682) >> 16, wc = (xcd & 1) * 14 + m - 14 * wr;
      unsigned o = (w32 * 12 + (lane < 12 ? lane : 0)) * 128;
      asm volatile("s_getpc_b64 s[40:41]\n91:\n\ts_sub_u32 s40, s40, 91b-.Lcode_begin\n\ts_subb_u32 s41, s41, 0\n\tv_min_u32 %0, .Lffn_code_end-.Lcode_begin, %0\n\t"
                   "s_mov_b32 m0, 0x20000\n\ts_nop 0\n\tglobal_load_lds_dword %0, s[40:41]" : "+v"(o) :: "s40", "s41", "scc", "memory");
      const char* p = n < 512 ? X + ((xcd >> 1) * 512 + n) * 1152 : W + wr * 3456 + (wc < 27 ? wc : 26) * 128; const int st = n < 512 ? 128 : 64 * 3456;
#pragma unroll
      for (int s = PFS; s < 9; ++s) asm volatile("global_load_lds_dword %0, off" :: "v"(p + s * st) : "memory"); }
    u32x4 rs, dst; const int e = sp * 128 + (tid & 127);
    if (wid < 4) retile_plan((e & 1) * 256 + ((e >> 1) & 255), e >> 9, WG, WU, WD, rs, dst);
    tag = load_tag(ws); rs[3] = tag;
    asm volatile("s_waitcnt vmcnt(%c0)" :: "i"(10 - PFS) : "memory");
    if (ks < 18) {
      const int slot = wid * 4 + g; LAS const u32x2* rp = (LAS const u32x2*)(lds + 36864 + wid * 5120 + g * 1152) + l15;
      float s = 0.f;
#pragma unroll
      for (int i = 0; i < 9; ++i) { const u32x2 u = rp[i * 16]; s += (bflo(u[0]) + bfhi(u[0])) + (bflo(u[1]) + bfhi(u[1])); }
      s += __shfl_xor(s, 1); s += __shfl_xor(s, 2); s += __shfl_xor(s, 4); s += __shfl_xor(s, 8);
      s *= (1.f / 576.f);
      const unsigned hi = cvtpk(s, 0.f) & 0xffffu, lo = cvtpk(s - bflo(hi), 0.f) & 0xffffu;
      *(LAS unsigned short*)(lds + 32768 + ((slot >> 3) * 16 + l15) * 16 + (slot & 7) * 2) = (l15 & 3) == 0 ? hi : (l15 & 3) == 1 ? lo : 0u;
    }
    LBAR();
    if (job < 162) {
      LAS const char* rp = lds + wid * 4096 + (lane >> 4) * 1024 + ((lane >> 2) & 3) * 128 + (lane & 3) * 16;
#pragma unroll
      for (int i = 0; i < 4; ++i) {
        const u32x2 lo = trread(rp + (i >> 1) * 64 + (i & 1) * 8), hi = trread(rp + 512 + (i >> 1) * 64 + (i & 1) * 8);
        st16(ws + WS_WO + ((4 * cb + i) * 18 + wk) * 1024 + lane * 16, u32x4{lo[0], lo[1], hi[0], hi[1]});
      }
    }
    static_assert(FLAG_BYTES == 32 * 512 * 16, "one store per thread");
    st16(ws + WS_FL + (sp * 512 + tid) * 16, u32x4{0, 0, 0, 0});
    if (wid < 4) st16(ws + WS_PL + (wid >> 1) * 65536 + e * 16, wid < 2 ? rs : dst);
    if (wid == 4 && ks < 18) st16(ws + WS_WB + ks * 1024 + lane * 16, *(LAS const u32x4*)(lds + 32768 + lane * 16));
    if (wid == 5) st16(ws + WS_KV + (2 * sp + (lane >> 5)) * G_TS + 36864 + ((lane >> 3) & 3) * V_GS + 9216 + (lane & 7) * 16, u32x4{0x3f803f80u, 0x3f803f80u, 0x3f803f80u, 0x3f803f80u});
    asm volatile("s_waitcnt vmcnt(0) lgkmcnt(0)\n\ts_barrier" ::: "memory");
    if (tid == 64) st8(ws + WS_FQ + (224 + sp) * 128, u32x2{tag, ~tag});
    return;
  }
  const int rt = (xcd >> 1) * 4 + (j & 3), ct = (xcd & 1) * 7 + (j >> 2), tgw = wid & 3, un = wid >> 2, unit = ct * 2 + un, type = unit / 9;
  const bool vform = ct >= 9;
  const int r8 = lane >> 3, c8 = lane & 7, vm = vform ? -1 : 0;
  const int xrow = 32 * (wid >> 1) + (((8 * (r8 >> 2) + 4 * (wid & 1) + (r8 & 3)) & vm) | ((2 * r8 + (wid & 1)) & ~vm));
  const unsigned vx0 = __umul24(xrow, 1152) + (c8 ^ (r8 >> 1)) * 16;
  u32x4 vld = {vx0, (vx0 ^ 64) + 16 * 1152, (unsigned)__umul24(8 * wid + r8, 3456) + (c8 ^ ((((r8 >> 1) & 1) | ((wid & 1) << 1)) << 1)) * 16, 0u};
  u32x8 sd, sc = uni(u32x8{0u, (unsigned)ct * 256, (unsigned)rt * 128 * 1152, 0u, (unsigned)wid * 2048, 16384u + wid * 1024, (unsigned)ct, 0u});
  asm volatile("" : "+{v[56:59]}"(vld)); asm volatile("" : "+{s[72:79]}"(sc)); SB();
  asm volatile(QKV_ASM0 : "=&{s[64:71]}"(sd), "+{s[72:79]}"(sc) : "{v[56:59]}"(vld), [x] "s"(X), [w] "s"(W) : "memory", "scc", "s80", "s81", "s82"); SB();
  const unsigned ax = (tgw * 32 + un * 16 + l15) * 128 + (g ^ (l15 >> 1)) * 16;
  u32x8 vrd; vrd[0] = ax; vrd[1] = ax ^ 2048; vrd[2] = ax ^ 64; vrd[3] = ax ^ (2048 + 64);
  { const int q = (lane >> 2) & 3; const unsigned aw = 16384 + un * 8192 + (8 * g + q) * 128 + ((q >> 1) | ((g & 1) << 1)) * 32 + (lane & 3) * 8;
#pragma unroll
    for (int i = 0; i < 4; ++i) vrd[4 + i] = aw ^ (((i + tgw) & 3) * 32); }
  const unsigned tile = rt * 4 + tgw, ub = unit - 9 * type, p0 = tgw * 32 + un * 16, cb = un * 512 + tgw * 64; const bool diag = g == (l15 >> 2);
  constexpr unsigned ST = 131072, CSUM = ST + 1024;
  const unsigned ob = sel(unit < 27, sel(type == 0, (unsigned)WS_Q + tile * Q_TS, (unsigned)WS_KV + tile * G_TS) + sel(vform, 36864 + ub * 1024, ub * 4096), OUTSIDE) + __umul24(g, sel(vform, V_GS, 256)) + l15 * 16;
  u32x16 ve;
#pragma unroll
  for (int i = 0; i < 4; ++i) ve[i] = ob + ((i + tgw) & 3) * sel(vform, 256, 1024);
  ve[5] = sel(diag, ST + ((p0 + l15) << sel(vform, 2, 3)), 0x40000u); ve[6] = sel(l15 == 0, CSUM + cb + g * 16, 0x40000u);
  ve[7] = sel(diag, sel(ct == 0, (unsigned)WS_MUX + (rt * 128 + tgw * 32 + un) * 4, OUTSIDE) + l15 * 8, OUTSIDE);
  ve[8] = ST + sel(vform, p0 * 4 + g * 16, (p0 + l15) * 8); ve[9] = ve[8] ^ sel(vform, 64, 128); ve[10] = CSUM + cb + sel(vform, l15 * 4, g * 16);
  const i32x4 rws = mkrsrc(ws, (unsigned)WS_END);
  const u32x8 se = uni(u32x8{(unsigned)rws[0], (unsigned)rws[1], (unsigned)rws[2], (unsigned)rws[3], 0u - un, 0u - un, sel(type == 0, 0x3eae1a92u  , 0x3f800000u), 0u});
  asm volatile(QKV_ASM1 : "+{s[64:71]}"(sd), "+{s[72:79]}"(sc), "+{v[48:55]}"(vrd), [tag] "=&s"(tag)
    : "{v[56:59]}"(vld), "{v[32:47]}"(ve), "{s[56:63]}"(se), [ep] "s"(ws + WS_EP) : "memory", "scc", QKV_CLOB);
}

DEVI void attn_exact(f32x16& acc, const char* qimg, const char* kv, int u, int lane) {
  __builtin_amdgcn_fence(__ATOMIC_ACQUIRE, "agent");
  const int h = lane >> 5, l31 = lane & 31, avt = 36864 + h * V_GS + (l31 < 18 ? (18 * u + l31) * 16 : 9216);
  const int akm = (u >> 1) * 2304 + (u & 1) * 1280 + h * 512 + l31 * 8, ake = (u >> 1) * 2304 + 1024 + l31 * 8 + (u & 1) * 4;
  const u32x2 q0 = *(const u32x2*)(qimg + akm), q1 = *(const u32x2*)(qimg + akm + 256), qe = {h ? 0u : *(const unsigned*)(qimg + ake), 0u};
  const u32x4 qm = {q0[0], q0[1], q1[0], q1[1]};
  acc = f32x16{}; float mx = -INFINITY;
#pragma unroll 1
  for (int t = 0; t < 64; ++t) {
    const char* p = kv + t * G_TS;
    const u32x2 k0 = *(const u32x2*)(p + akm), k1 = *(const u32x2*)(p + akm + 256), ke = {h ? 0u : *(const unsigned*)(p + ake), 0u};
    const u32x4 km = {k0[0], k0[1], k1[0], k1[1]}, v0 = *(const u32x4*)(p + avt), v1 = *(const u32x4*)(p + 2 * V_GS + avt);
    f32x16 s = __builtin_amdgcn_mfma_f32_32x32x16_bf16(__builtin_bit_cast(bf16x8, km), __builtin_bit_cast(bf16x8, qm), f32x16{}, 0, 0, 0);
    s = __builtin_amdgcn_mfma_f32_32x32x8bf16_1k(__builtin_bit_cast(s16x4, ke), __builtin_bit_cast(s16x4, qe), s, 0, 0, 0);
    float m = mx;
#pragma unroll
    for (int r = 0; r < 16; ++r) m = fmaxf(m, s[r]);
    m = fmaxf(m, __shfl_xor(m, 32));
    acc *= __builtin_amdgcn_exp2f(mx - m); mx = m;
    f32x8 p0, p1;
#pragma unroll
    for (int r = 0; r < 8; ++r) { p0[r] = __builtin_amdgcn_exp2f(s[r] - m); p1[r] = __builtin_amdgcn_exp2f(s[r + 8] - m); }
    acc = __builtin_amdgcn_smfmac_f32_32x32x32_bf16(__builtin_bit_cast(bf16x8, v0), __builtin_bit_cast(bf16x16, p0), acc, 0xdddd, 0, 0);
    acc = __builtin_amdgcn_smfmac_f32_32x32x32_bf16(__builtin_bit_cast(bf16x8, v1), __builtin_bit_cast(bf16x16, p1), acc, 0xdddd, 0, 0);
  }
}
DEVI u32x4 wo_a(const u32x32& wa0, const u32x32& wa1, const u32x8& wa2, int i) {
  u32x4 a;
#pragma unroll
  for (int e = 0; e < 4; ++e) a[e] = i < 8 ? wa0[(i & 7) * 4 + e] : i < 16 ? wa1[(i & 7) * 4 + e] : wa2[(i & 1) * 4 + e];
  return a; }
DEVI void wo_out(i32x4 rws, f32x4 v0, f32x4 v1, float mu0, float mu1, int otg, int tile, int lane) {
  bst16(rws, (unsigned)WS_HC + (otg * 36 + tile) * 1024 + lane * 16, u32x4{cvtpk(v0[0], v0[1]), cvtpk(v0[2], v0[3]), cvtpk(v1[0], v1[1]), cvtpk(v1[2], v1[3])});
  const float s0 = (v0[0] * v0[0] + v0[1] * v0[1]) + (v0[2] * v0[2] + v0[3] * v0[3]), s1 = (v1[0] * v1[0] + v1[1] * v1[1]) + (v1[2] * v1[2] + v1[3] * v1[3]);
  const auto w = __builtin_amdgcn_permlane16_swap(__float_as_uint(s0), __float_as_uint(s1), false, false);
  const unsigned h = __float_as_uint(__uint_as_float(w[0]) + __uint_as_float(w[1]));
  const auto z = __builtin_amdgcn_permlane32_swap(h, h, false, false);
  const float sq = __uint_as_float(z[0]) + __uint_as_float(z[1]);
  bst4(rws, sel(lane < 32, (unsigned)WS_SSQ + tile * 8192, sel(tile, OUTSIDE, (unsigned)WS_SSQ + 36 * 8192)) + otg * 128 + (lane & 31) * 4, sel(lane < 32, __float_as_uint(sq), sel(lane & 16, __float_as_uint(mu1), __float_as_uint(mu0))));
}
DEVI f32x4 unpk(unsigned a, unsigned b) { return f32x4{bflo(a), bfhi(a), bflo(b), bfhi(b)}; }

__global__ void __launch_bounds__(512, 2) k_main(const char* X, const char* WQKV, const char* WO, const char* WG, const char* WU, const char* WD, char* ws, char* out) {
  asm volatile(".Lcode_begin:");
  extern __shared__ __attribute__((aligned(16))) char lds_[];
  LAS char* lds = (LAS char*)lds_;
  const int wid = __builtin_amdgcn_readfirstlane(threadIdx.x >> 6); __builtin_assume(wid >= 0 && wid < 8);
  unsigned tag;
  {
    phase_qkv(X, WQKV, WO, WG, WU, WD, ws, lds, wid, tag);
  }
  X = own(X); ws = own(ws); out = own(out);
  int bid; asm volatile("s_mov_b32 %0, %1" : "=s"(bid) : "s"(blockIdx.x)); __builtin_assume(bid >= 0 && bid < 256);
  const int xcd = bid & 7, j = bid >> 3;
  const int rb = (xcd & 1) * 4 + (j & 3), k4 = xcd >> 1, u = k4 * 8 + (j >> 2);
  const i32x4 rws = mkrsrc(ws, (unsigned)WS_END);
  const int otg = rb * 8 + (u >> 2), ot = (u & 3) * 9 + wid, ot9 = (u & 3) * 9 + 8;
  u32x32 wa0, wa1; u32x16 vin; u32x8 wa2; u32x4 xr; unsigned frw;
  {
    const int lane = lane_id(), qb = wid & 3, kh = wid >> 2, h = lane >> 5, l31 = lane & 31;
    const int tg = rb * 8 + qb * 2 + kh;
    const unsigned akm = (u & 1) * 256 + h * 512 + l31 * 8, ake = sel(u & 1, 4, 1024) + l31 * 8, ho = (u >> 1) * 2304 + (u & 1) * 1024;
    const unsigned row = sel(l31 == 22, 18, l31), mi = ((u & 3) * 8 + rb) * 8 + wid, pa = 9216, pb = sel(mi < 68, 9216, 1152), vo = lane * 16, l15 = lane & 15, g = lane >> 4;
    const unsigned stg = wid * 4096, stp = (wid ^ 4) * 4096, xo = (otg * 32 + l15) * 1152 + g * 16, rc = (lane & 7) * 16;
    vin = u32x16{vo, akm, ake, sel(row > 18, 0x40000u, h * 304 + row * 16),
                 sel(lane == 0, sel((bid == 255) & (wid == 0), WS_EP, OUTSIDE), OUTSIDE), (unsigned)(j * 8 + wid) * 128, (unsigned)WS_PL + ((u >> 2) * 256 + mi) * 32 + (lane & 8) * 8192 + (lane & 7) * 4, stp + vo,
                 (lane >> 3) * pa + rc, (unsigned)__umul24(lane >> 3, pb) + rc, stg + g * 1024 + (lane & 12) * 32 + (lane & 3) * 16, stg + vo,
                 xo + (ot9 >> 1) * 64 + (ot9 & 1) * 8, xo + (ot >> 1) * 64 + (ot & 1) * 8, (unsigned)WS_MUX + otg * 128 + l15 * 4, (unsigned)WS_FA + (wid * 32 + rb) * 128 + (lane & 3) * 1024};
    xr = u32x4{tag, ~tag, 0u, 0u};
    { unsigned fo = sel(lane == 0, sel((wid == 2) & (j < 28), (unsigned)WS_FQ + (((xcd >> 1) * 4 + (j & 3)) * 14 + (xcd & 1) * 7 + (j >> 2)) * 128, OUTSIDE), OUTSIDE);
      SB(); asm volatile("s_waitcnt vmcnt(0) lgkmcnt(0)\n\ts_barrier\n\tbuffer_store_dwordx2 v[4:5], %2, %3, 0 offen sc1" : "+{v[16:31]}"(vin), "+{v[4:7]}"(xr) : "v"(fo), "s"(rws) : "memory");
      SB(); }
    u32x8 s0 = {(unsigned)(size_t)X, (unsigned)((size_t)X >> 32), (unsigned)WS_Q + ho + (rb * 8 + wid) * Q_TS, sel(wid, WS_END, (unsigned)WS_FR + (u * 8 + rb) * 128), (unsigned)rws[0], (unsigned)rws[1], (unsigned)rws[2], (unsigned)rws[3]};
    const unsigned xd = (35 - ot9) * 18 * 1024, ibw = (unsigned)WS_IB + bid * IB_S;
    u32x16 s1 = {(unsigned)wid * K_TS, ibw + wid * 3072, sel(qb == k4, ibw + 24576 + kh * 1536, 0x7f000000u), (unsigned)WS_WO + (ot * 18 + (kh ^ 1) * 9) * 1024, (unsigned)(size_t)ws, (unsigned)((size_t)ws >> 32), (unsigned)WS_KV + wid * G_TS, (unsigned)WS_WO + (ot9 * 18 + wid) * 1024,
                 (unsigned)kh * K_TS, 8192 + sel(wid >= 2, xd, 0), 8192 + sel(wid < 2, xd, 0), 56u * K_TS + wid * 1280, pa * 8, pb * 8, 32768u + wid * 1024, (unsigned)WS_WO + (ot * 18 + kh * 9) * 1024};
    const unsigned qb0 = 56 * K_TS + (tg & 7) * 1280, qb1 = 56 * K_TS + ((tg ^ 1) & 7) * 1280;
    const auto vsrc = [&](unsigned i) { const unsigned gg = (i * 27) >> 9, d = i - 19 * gg; return 36864 + gg * V_GS + sel(d < 18, (18 * u + d) * 16, 9216); };
    const unsigned f0 = l15 * 14 + ((576 + h * 576 + 18 * u + (g & 1) * 17) >> 7), f1 = sel(h, 192 + lane, (2 * rb + (lane & 1)) * 14 + (sel(lane & 4, 0, 18 * u + ((lane >> 1) & 1) * 17) >> 7));
    wa2 = u32x8{qb0 + akm, qb1 + akm, sel(h, qb1, qb0) + ake, ho + vo, sel(lane < 16, ho + vo, vsrc((lane - 16) & 63) - 1024), vsrc(48 + sel(lane < 28, lane, 0)) - 2048,
                (unsigned)WS_FQ + (bid & 31) * 7 * 128, (unsigned)WS_FQ + sel(wid >= 2, ((wid - 2) & 3) * 64 + lane, sel(wid, f1, f0)) * 128};
    const unsigned po = (unsigned)WS_OT + (rb * 32 + u) * 9216, ob = (unsigned)WS_OT + (tg * 18 + (u >> 4) * 9) * 2048;
    const u32x4 s2 = uni(u32x4{po + wid * 1024, sel(wid, 0x7f000000u, po + 8192), (unsigned)(WS_FA - WS_FR), ((sel(bid == 255, 0x3f, 3) >> wid) & 1u) * sel(j >= 28, 3, 1)});
    unsigned om = ob + ((u >> 1) & 7) * 2048 + (g & 1) * 1024 + (2 * (u & 1) + h) * 256 + l15 * 16, oe = sel(h, OUTSIDE, ob + 8 * 2048 + (u & 15) * 128 + l31 * 4);
    asm volatile("" : "+v"(om), "+v"(oe));
    s0 = uni(s0); s1 = uni(s1);
    f32x16 o;
    asm volatile(ATTN_ASM
      : "={v[192:207]}"(o), "={v[48:79]}"(wa0), "={v[80:111]}"(wa1), "+{v[8:15]}"(wa2), "+{v[4:7]}"(xr), "+{v[16:31]}"(vin), "+{s[56:63]}"(s0), "+{s[64:79]}"(s1)
      : "{s[96:99]}"(s2) : "memory", "scc", "vcc", ATTN_CLOB);
    float am = 0.f;
#pragma unroll
    for (int r = 0; r < 10; ++r) am += fabsf(o[r]);
    const bool bad = !(o[10] > 0x1p-60f && o[10] < 0x1p120f && am < 0x1p126f);
    if (__builtin_expect(__any(bad), 0)) attn_exact(o, ws + WS_Q + tg * Q_TS, ws + WS_KV, u, lane_id());
#define PKM(x, y) min(cvtpk(x, y), 0xfffffffeu)
    const float inv = __builtin_amdgcn_rcpf(o[10]);
    bst16(rws, om, u32x4{PKM(o[0] * inv, o[1] * inv), PKM(o[2] * inv, o[3] * inv), PKM(o[4] * inv, o[5] * inv), PKM(o[6] * inv, o[7] * inv)});
    bst4(rws, oe, PKM(o[8] * inv, o[9] * inv));
  }
  const int cg = (j >> 2) & 3, kh = wid >> 2, wv = (j >> 4) * 4 + (wid & 3), tg = rb * 8 + wv, so = wid & 3;
  const int bp = (cg >> 1) * 9 + (cg & 1) * 5 + k4, bs = (cg >> 1) * 9 + 4;
  u32x16 si, sj; u32x8 vi; u32x4 vf;
  {
    const int lane = lane_id(), l15 = lane & 15, g = lane >> 4, voff = lane * 16, te = wid & 1;
    constexpr int OB = 102400, XB = 94208;
    const int hf = wid >> 2; u32x4 ex[9];
#pragma unroll
    for (int i = 0; i < 9; ++i) ex[i] = *(LAS const u32x4*)(lds + 32768 + (((wid >> 1) & 1) * 18 + hf * 9 + i) * 1024 + voff);
    f32x4 acc[2], xacc;
    { const f32x4 r0 = unpk(xr[0], xr[1]), r1 = unpk(xr[2], xr[3]); acc[0] = te ? r1 : r0; acc[1] = te ? r0 : r1; asm volatile("" : "+v"(acc[0]), "+v"(acc[1])); }
    unsigned pb[2][2], px = XB + voff, pw_ = XB + wid * 1024 + voff;
#pragma unroll
    for (int q = 0; q < 4; ++q) { pb[q >> 1][q & 1] = OB + ((q >> 1) ^ hf) * 18432 + voff + ((q & 1) ^ te) * 1024; asm volatile("" : "+v"(pb[q >> 1][q & 1])); }
    asm volatile("" : "+v"(px), "+v"(pw_));
    { const int mo = (wid >> 2) * 18432 + (wid & 3) * 4096 + voff, eo = (wid >> 2) * 18432 + 16384 + (wid & 3) * 512 + lane * 8;
      const int so_ = (int)WS_OT + otg * 36864; u32x16 d; u32x2 de;
#define O_LOADS "buffer_load_dwordx4 v[128:131], %2, %4, %5 offen sc1\n\tbuffer_load_dwordx4 v[132:135], %2, %4, %5 offen offset:1024 sc1\n\tbuffer_load_dwordx4 v[136:139], %2, %4, %5 offen offset:2048 sc1\n\t" \
                "buffer_load_dwordx4 v[140:143], %2, %4, %5 offen offset:3072 sc1\n\tbuffer_load_dwordx2 v[144:145], %3, %4, %5 offen sc1"
      SB(); __builtin_amdgcn_s_sleep(APRE);
      asm volatile(O_LOADS : "=&{v[128:143]}"(d), "=&{v[144:145]}"(de) : "v"(mo), "v"(eo), "s"(rws), "s"(so_) : "memory"); SB();
      {
          const unsigned lane = lane_id(), voff = lane * 16;
          const unsigned sw = (unsigned)WS_WGU + u * 324 * 1024 + wid * 1024, delta = (unsigned)WS_WD + (k4 * 4 + cg) * 324 * 1024 - ((unsigned)WS_WGU + (u + 1) * 324 * 1024);
          const unsigned hp = (unsigned)WS_HH + ((rb * 8 + wid) * 144 + (u >> 1) * 9) * 2048, ib = (unsigned)WS_IB + (bid & ~6) * IB_S + wid * 3072, is = (unsigned)WS_IB + (bid & ~6) * IB_S + 24576 + kh * 1536, msk = 0u - ((u >> 2) & 1);
          vi = u32x8{voff, voff + kh * 1024, voff + (kh ^ 1) * 1024, voff + kh * 8, (unsigned)WS_FC + ((rb * 8 + wv) * 32 + k4 * 8 + kh * 4 + (lane & 3)) * 32,
                      sel(wid < 6, 73728, 104448) + wid * 7168 + voff, sel((wid ^ 4) < 6, 73728, 104448) + (wid ^ 4) * 7168 + voff, (tg * 32 + kh * 16 + (lane & 15)) * 4 + (lane >> 4) * 8192};
          si = u32x16{(unsigned)rws[0], (unsigned)rws[1], (unsigned)rws[2], (unsigned)rws[3], sw, (unsigned)wid * 1024, 8192 + sel(wid >= 4, delta, 0), 8192 + sel(wid < 4, delta, 0),
                       (unsigned)WS_HC + (rb * 8 + wid) * 36864, (unsigned)WS_HH + (tg * 144 + k4 * 36 + kh * 18) * 2048, hp + (u & 1) * 5 * 2048, hp + 4 * 2048 + (u & 1) * 1024, 0u, (unsigned)WS_FC + ((rb * 8 + wid) * 32 + u) * 32, 0u, (unsigned)kh * 1024};
          sj = u32x16{msk, msk, (unsigned)WS_HC + tg * 36864 + bp * 2048, (unsigned)WS_HC + tg * 36864 + (2 * bs + (cg & 1)) * 1024, (unsigned)WS_SSQ,
                       ib + ((k4 + 1) & 3) * 2 * IB_S + 2048, ib + ((k4 + 2) & 3) * 2 * IB_S + 1024, ib + ((k4 + 3) & 3) * 2 * IB_S,
                       sel(so != k4, is + so * 2 * IB_S + ((k4 - so - 1) & 3) * 512, 0x7f000000u),
                       ib + k4 * 2 * IB_S, sel(so == k4, is + k4 * 2 * IB_S, 0x7f000000u), 0u, 1u << (wid & 3), 0u, 0u, 0u};
          vf = u32x4{(unsigned)WS_FR + (k4 * 64 + wid * 8 + (lane & 7)) * 128, sel(lane == 0, sel(wid == 0, (unsigned)WS_FB + (rb * 32 + u) * 128, OUTSIDE), OUTSIDE), (unsigned)WS_FB + (rb * 32 + wid * 4 + (lane & 3)) * 128, 0u};
          si = uni(si); sj = uni(sj);
          asm volatile("" : "+{s[64:79]}"(si), "+{s[80:95]}"(sj)); asm volatile("" : "+v"(vi), "+v"(vf));
      }
      unsigned lm = OB + mo, le = OB + (wid >> 2) * 18432 + 16384 + (l15 >> 3) * 1024 + (wid & 3) * 256 + (l15 & 7) * 32 + g * 4;
      SB(); asm volatile("s_waitcnt vmcnt(0)" : "+{v[128:143]}"(d), "+{v[144:145]}"(de), "+v"(lm), "+v"(le) :: "memory");
#pragma unroll 1
      for (int n = 0; n < (1 << 18); ++n) {
        unsigned m = max(de[0], de[1]);
#pragma unroll
        for (int e = 0; e < 16; e += 2) m = max(max(m, d[e]), d[e + 1]);
        if (__builtin_expect(__all(m != 0xffffffffu), 1)) break;
        asm volatile(O_LOADS "\n\ts_waitcnt vmcnt(0)" : "+{v[128:143]}"(d), "+{v[144:145]}"(de) : "v"(mo), "v"(eo), "s"(rws), "s"(so_) : "memory");
      }
#pragma unroll
      for (int q = 0; q < 4; ++q) *(LAS u32x4*)(size_t)(lm + q * 1024) = u32x4{d[4 * q], d[4 * q + 1], d[4 * q + 2], d[4 * q + 3]};
      LAS unsigned* ep = (LAS unsigned*)(size_t)le; ep[0] = de[0]; ep[4] = de[1]; }
    SB(); LBAR(); SB();
    u32x4 b[18][2]; f32x2 m[4]; float mu0, mu1;
#pragma unroll
    for (int i = -WRD; i < 18; ++i) {
      const int n = i + WRD;
      if (n < 18) {
#pragma unroll
        for (int t = 0; t < 2; ++t) b[n][t] = *(LAS const u32x4*)(size_t)(pb[n >= 9][t] + (n % 9) * 2048);
        SB();
      }
      if (i >= 0) {
        const u32x4 a = wo_a(wa0, wa1, wa2, i);
        if (i < 9) xacc = MFMA(ex[i], b[i][0], i == 0 ? f32x4{} : xacc);
        acc[0] = MFMA(a, b[i][0], acc[0]); acc[1] = MFMA(a, b[i][1], acc[1]);
        SB();
      }
      if (i == 9) { *(LAS f32x4*)(size_t)pw_ = xacc; SB(); }
      if (i == WBP) {
        LBAR(); SB();
#pragma unroll
        for (int q = 0; q < 4; ++q) { m[q] = *(LAS const f32x2*)(size_t)(px + (2 + (q & 1) + (q >> 1) * 4) * 1024); }
        frw = __builtin_amdgcn_raw_buffer_load_b32(__builtin_amdgcn_make_buffer_rsrc(ws, 0, (unsigned)WS_END, 0x00020000), vf[0], 0, 16  );
        SB();
      }
      if (i == WBP + 1) {
        mu0 = __uint_as_float(vin[4]) + (m[0][0] + m[0][1]) + (m[2][0] + m[2][1]); mu1 = __uint_as_float(vin[5]) + (m[1][0] + m[1][1]) + (m[3][0] + m[3][1]);
        if (wid == 3) { LAS const f32x4* xp = (LAS const f32x4*)(size_t)px;
          wo_out(rws, xp[0] + xp[256] + unpk(vin[2], vin[3]) - mu0, xp[64] + xp[320] + unpk(vin[12], vin[13]) - mu1, mu0, mu1, otg, ot9, lane); }
        SB();
      }
    }
    wo_out(rws, (te ? acc[1] : acc[0]) - mu0, (te ? acc[0] : acc[1]) - mu1, mu0, mu1, otg, ot, lane);
  }
  {
    f32x8 acc; f32x4 acc8; u32x8 tv, pw; u32x16 pv; f32x2 ms;
    {
      asm volatile(FFN_ASM : "={v[100:107]}"(acc), "={v[92:95]}"(acc8), "={v[112:119]}"(tv), "={v[120:135]}"(pv), "={v[136:143]}"(pw), "={v[0:1]}"(ms), "+{s[64:79]}"(si), "+{s[80:95]}"(sj), "+{v[16:19]}"(vf), "+{v6}"(frw) : "{v[8:15]}"(vi) : "memory", "scc", "vcc", FFN_CLOB);
    }
    const int lane = lane_id(), l15 = lane & 15, g = lane >> 4, tk = tg * 32 + kh * 16 + l15;
    const float r2 = __builtin_amdgcn_rcpf(gsum(ms[1]) * (1.f / 576.f) + 1e-5f), mu = ms[0];
    float bv[12];
#pragma unroll
    for (int e = 0; e < 6; ++e) { bv[2 * e] = bflo(tv[e]) + mu + r2 * (e < 4 ? acc[2 * e] : acc8[2 * e - 8]); bv[2 * e + 1] = bfhi(tv[e]) + mu + r2 * (e < 4 ? acc[2 * e + 1] : acc8[2 * e - 7]);
      asm volatile("" : "+v"(bv[2 * e]), "+v"(bv[2 * e + 1])); }
    const i32x4 ro = mkrsrc(out, 2048 * 1152);
    unsigned oo = tk * 1152 + bp * 64 + g * 16, oq = sel(so == k4, tk * 1152 + bs * 64 + g * 16 + (cg & 1) * 8, OUTSIDE);
    asm volatile("s_waitcnt vmcnt(0)" : "+{v[120:135]}"(pv), "+{v[136:143]}"(pw), "+v"(oo), "+v"(oq) :: "memory");
#pragma unroll 1
    for (int n = 0; n < (1 << 18); ++n) {
      unsigned m = max(pw[0], pw[1]);
#pragma unroll
      for (int k = 0; k < 16; k += 2) m = max(max(m, pv[k]), pv[k + 1]);
      if (__builtin_expect(__all(m != 0xffffffffu), 1)) break;
      const unsigned ln = lane_id(), ib = (unsigned)WS_IB + bid * IB_S + wid * 3072;
      asm volatile("buffer_load_dwordx4 v[120:123], %2, %4, 0 offen sc1\n\tbuffer_load_dwordx4 v[124:127], %2, %4, 0 offen offset:1024 sc1\n\tbuffer_load_dwordx4 v[128:131], %2, %4, 0 offen offset:2048 sc1\n\t"
                   "buffer_load_dwordx2 v[132:133], %3, %4, 0 offen sc1\n\tbuffer_load_dwordx2 v[134:135], %3, %4, 0 offen offset:512 sc1\n\tbuffer_load_dwordx2 v[136:137], %3, %4, 0 offen offset:1024 sc1\n\ts_waitcnt vmcnt(0)"
        : "+{v[120:135]}"(pv), "+{v[136:143]}"(pw) : "v"(ib + ln * 16), "v"(sel(so == k4, (unsigned)WS_IB + bid * IB_S + 24576 + kh * 1536, OUTSIDE) + ln * 8), "s"(rws) : "memory");
    }
    u32x4 o; u32x2 os;
#pragma unroll
    for (int e = 0; e < 4; ++e) o[e] = cvtpk(bv[2 * e] + r2 * (bflo(pv[e]) + bflo(pv[4 + e]) + bflo(pv[8 + e])), bv[2 * e + 1] + r2 * (bfhi(pv[e]) + bfhi(pv[4 + e]) + bfhi(pv[8 + e])));
    asm volatile("buffer_store_dwordx4 %0, %1, %2, 0 offen\n\ts_nop 1" ::"v"(o), "v"(oo), "s"(ro) : "memory");
#pragma unroll
    for (int e = 0; e < 2; ++e) os[e] = cvtpk(bv[8 + 2 * e] + r2 * (bflo(pv[12 + e]) + bflo(pv[14 + e]) + bflo(pw[e])), bv[9 + 2 * e] + r2 * (bfhi(pv[12 + e]) + bfhi(pv[14 + e]) + bfhi(pw[e])));
    asm volatile("buffer_store_dwordx2 %0, %1, %2, 0 offen\n.Lffn_code_end:" ::"v"(os), "v"(oq), "s"(ro) : "memory");
  }
}

extern "C" void kernel_launch(void* const* d_in, const int* in_sizes, int n_in, void* d_out, int out_size, void* d_ws, size_t ws_size, hipStream_t stream) {
  const char* x = (const char*)d_in[0]; const char* wqkv = (const char*)d_in[1]; const char* wo = (const char*)d_in[2];
  const char* wg = (const char*)d_in[4]; const char* wu = (const char*)d_in[5]; const char* wd = (const char*)d_in[6];
  char* ws = (char*)d_ws;
  (void)hipFuncSetAttribute((const void*)k_main, hipFuncAttributeMaxDynamicSharedMemorySize, LDS_BYTES);
  k_main<<<256, 512, LDS_BYTES, stream>>>(x, wqkv, wo, wg, wu, wd, ws, (char*)d_out);
}
```
